```python
import torch

def _softmax(x, axis=-1):
    return torch.softmax(x, dim=axis)
def _gelu(x):
    return torch.nn.functional.gelu(x, approximate="tanh")
def _silu(x):
    return torch.nn.functional.silu(x)
def _sigmoid(x):
    return torch.sigmoid(x)
def _layernorm(x, axis=-1, eps=1e-5):
    m = x.mean(dim=axis, keepdim=True)
    v = x.var(dim=axis, keepdim=True, unbiased=False)
    return (x - m) / torch.sqrt(v + eps)
def _scatter_add(tgt, idx, src, axis=0):
    return tgt.clone().index_add_(axis, idx, src)
def _segment_sum(src, seg, n):
    out = torch.zeros((n,) + src.shape[1:], dtype=src.dtype)
    return out.index_add_(0, seg, src)
def _pad(x, pw):
    flat = tuple(v for pair in reversed(pw) for v in pair)
    return torch.nn.functional.pad(x, flat)

kernel_name = 'gated_mix__sliding_block__dense_activation_relu__prompt'

def setup_inputs():

    _w = (lambda a: (lambda t: t.to(torch.bfloat16) if t.is_floating_point() else t)(torch.as_tensor(a)))
    def _A_setup():
        B, S, D = 1, 2048, 448
        return {
            "x": _w((lambda *s: torch.randn(*s, dtype=torch.bfloat16) * 0.05)(B, S, D)),
            "wqkv": _w((lambda *s: torch.randn(*s, dtype=torch.bfloat16) * 1.6)(D, 3 * D)),
            "wo": _w((lambda *s: torch.randn(*s, dtype=torch.bfloat16) * 0.05)(D, D)),
        }
    _A_in = _A_setup()

    def _B_setup():
        B, S, D, Dff = 1, 2048, 448, 1792
        return {
            "x": _w((lambda *s: torch.randn(*s, dtype=torch.bfloat16) * 0.05)(B, S, D)),
            "w1": _w((lambda *s: torch.randn(*s, dtype=torch.bfloat16) * 0.2)(D, Dff)),
            "w2": _w((lambda *s: torch.randn(*s, dtype=torch.bfloat16) * 0.2)(Dff, D)),
        }
    _B_in = _B_setup()

    return {
        "A_x": _A_in["x"],
        "A_wqkv": _A_in["wqkv"],
        "A_wo": _A_in["wo"],
        "B_x": _B_in["x"],
        "B_w1": _B_in["w1"],
        "B_w2": _B_in["w2"],
        "gate_w": _w((lambda *s: torch.randn(*s, dtype=torch.bfloat16) * 0.05)(448) * 16.0),
    }

def reference(A_x, A_wqkv, A_wo, B_x, B_w1, B_w2, gate_w):
    def _A_ref(x, wqkv, wo):
        B, S, D = x.shape
        H = 16; Dh = D // H; W = 256
        qkv = (lambda x, *d: x.permute(*d))((x @ wqkv).reshape(B, S, 3, H, Dh), 2, 0, 3, 1, 4)
        q, k, v = qkv[0], qkv[1], qkv[2]
        scores = torch.einsum("bhqd,bhkd->bhqk", q, k) * (Dh ** -0.5)
        idx = torch.arange(S)
        mask = (idx[:, None] - idx[None, :] >= 0) & (idx[:, None] - idx[None, :] < W)
        scores = torch.where(mask[None, None], scores, -1e9)
        p = _softmax(scores, axis=-1)
        o = torch.einsum("bhqk,bhkd->bhqd", p, v)
        return (lambda x, *d: x.permute(*d))(o, 0, 2, 1, 3).reshape(B, S, D) @ wo

    def _B_ref(x, w1, w2):
        h = torch.einsum("bsd,df->bsf", x, w1)
        zero = torch.zeros_like(h)
        h = torch.maximum(h, zero)
        return torch.einsum("bsf,fd->bsd", h, w2)

    x = A_x
    g = _sigmoid((x * gate_w).sum(-1, keepdims=True))
    a = _A_ref(x, A_wqkv, A_wo)
    b = _B_ref(x, B_w1, B_w2)
    return g * a + (1 - g) * b
```

```cpp
#include <hip/hip_runtime.h>
#include <hip/hip_bf16.h>
typedef __hip_bfloat16 bf16;
#define S_ 2048
#define D_ 448
#define H_ 16
#define DH 28
#define W_ 256
#define DFF 1792

__device__ inline float b2f(bf16 x){ return __bfloat162float(x);}
__device__ inline bf16 f2b(float x){ return __float2bfloat16(x);}

__global__ void gemm_naive(const bf16* A, const bf16* B, bf16* C, int M, int N, int K, int relu){
  int n = blockIdx.x*blockDim.x+threadIdx.x; int m = blockIdx.y;
  if(n>=N) return;
  float acc=0.f;
  for(int k=0;k<K;k++) acc += b2f(A[(size_t)m*K+k])*b2f(B[(size_t)k*N+n]);
  if(relu) acc = fmaxf(acc,0.f);
  C[(size_t)m*N+n]=f2b(acc);
}
__global__ void attn_naive(const bf16* qkv, bf16* o){
  int q = blockIdx.x; int h = blockIdx.y; int t = threadIdx.x;
  __shared__ float p[256]; __shared__ float red[256];
  int key = q - t; float s = -1e30f;
  if(key>=0){ float acc=0; for(int d=0;d<DH;d++) acc += b2f(qkv[(size_t)q*3*D_ + h*DH+d])*b2f(qkv[(size_t)key*3*D_ + D_ + h*DH+d]); s = acc*rsqrtf((float)DH);}
  red[t]=s; __syncthreads();
  for(int o2=128;o2>0;o2>>=1){ if(t<o2) red[t]=fmaxf(red[t],red[t+o2]); __syncthreads(); }
  float m=red[0]; __syncthreads();
  float e = key>=0? __expf(s-m):0.f; p[t]=e; red[t]=e; __syncthreads();
  for(int o2=128;o2>0;o2>>=1){ if(t<o2) red[t]+=red[t+o2]; __syncthreads(); }
  float sum=red[0];
  if(t<DH){ float acc=0; for(int j=0;j<256;j++){ int kk=q-j; if(kk>=0) acc += p[j]*b2f(qkv[(size_t)kk*3*D_+2*D_+h*DH+t]); } o[(size_t)q*D_+h*DH+t]=f2b(acc/sum);}
}
__global__ void combine(const bf16* x, const bf16* gw, const bf16* a, const bf16* b, bf16* out){
  int r = blockIdx.x; int t=threadIdx.x; __shared__ float red[512];
  float v = t<D_ ? b2f(x[(size_t)r*D_+t])*b2f(gw[t]) : 0.f; red[t]=v; __syncthreads();
  for(int o2=256;o2>0;o2>>=1){ if(t<o2) red[t]+=red[t+o2]; __syncthreads(); }
  float g = 1.f/(1.f+__expf(-red[0]));
  if(t<D_) out[(size_t)r*D_+t]=f2b(g*b2f(a[(size_t)r*D_+t])+(1.f-g)*b2f(b[(size_t)r*D_+t]));
}
extern "C" void kernel_launch(void* const* d_in, const int* in_sizes, int n_in, void* d_out, int out_size, void* d_ws, size_t ws_size, hipStream_t stream){
  const bf16* Ax=(const bf16*)d_in[0]; const bf16* wqkv=(const bf16*)d_in[1]; const bf16* wo=(const bf16*)d_in[2];
  const bf16* Bx=(const bf16*)d_in[3]; const bf16* w1=(const bf16*)d_in[4]; const bf16* w2=(const bf16*)d_in[5]; const bf16* gw=(const bf16*)d_in[6];
  bf16* ws=(bf16*)d_ws; bf16* qkv=ws; bf16* o=qkv+S_*3*D_; bf16* a=o+S_*D_; bf16* h=a+S_*D_; bf16* b=h+S_*DFF;
  gemm_naive<<<dim3((3*D_+255)/256,S_),256,0,stream>>>(Ax,wqkv,qkv,S_,3*D_,D_,0);
  attn_naive<<<dim3(S_,H_),256,0,stream>>>(qkv,o);
  gemm_naive<<<dim3((D_+255)/256,S_),256,0,stream>>>(o,wo,a,S_,D_,D_,0);
  gemm_naive<<<dim3((DFF+255)/256,S_),256,0,stream>>>(Ax,w1,h,S_,DFF,D_,1);
  gemm_naive<<<dim3((D_+255)/256,S_),256,0,stream>>>(h,w2,b,S_,D_,DFF,0);
  combine<<<S_,512,0,stream>>>(Ax,gw,a,b,(bf16*)d_out);
}
```

```cpp
#include <hip/hip_runtime.h>
#include <hip/hip_bf16.h>
#include <utility>
#include <type_traits>

typedef __hip_bfloat16 bf16;
typedef short bf16x8 __attribute__((ext_vector_type(8)));
typedef short s16x4 __attribute__((ext_vector_type(4)));
typedef float f32x4 __attribute__((ext_vector_type(4)));
typedef float f32x2 __attribute__((ext_vector_type(2)));
typedef float f32x16 __attribute__((ext_vector_type(16)));
typedef unsigned u32x4 __attribute__((ext_vector_type(4)));
typedef unsigned u32x2 __attribute__((ext_vector_type(2)));
typedef __bf16 bf16x2_t __attribute__((ext_vector_type(2)));
#define LAS __attribute__((address_space(3)))
#define GAS __attribute__((address_space(1)))
#define DI __device__ __forceinline__

constexpr int S_ = 2048, D_ = 448, NQKV = 1344, DFF = 1792;
constexpr size_t WS_FLAG = 0;
constexpr size_t WS_G    = 65536;
constexpr size_t WS_Q    = 131072;
constexpr size_t FRAG_TB = 28 * 1024;
constexpr size_t WS_K    = WS_Q + 64 * FRAG_TB;
constexpr size_t WS_V    = WS_K + 64 * FRAG_TB;
constexpr size_t WS_H    = WS_V + 64 * FRAG_TB + 4096;
constexpr size_t WS_O    = WS_H + 64 * 112 * 1024;

template <int I, int N, class F> DI void static_for(F&& f) {
  if constexpr (I < N) { f(std::integral_constant<int, I>{}); static_for<I + 1, N>(f); }
}
template <int N> DI void wait_vm() { asm volatile("s_waitcnt vmcnt(%0)" ::"n"(N) : "memory"); }
DI void wait_lgkm0() { asm volatile("s_waitcnt lgkmcnt(0)" ::: "memory"); }
DI void raw_barrier() { asm volatile("s_barrier" ::: "memory"); }
DI void glds16(const void* sbase, unsigned voff, unsigned lds_addr) {
  unsigned keep;
  asm volatile("s_mov_b32 %0, m0\n\ts_mov_b32 m0, %3\n\ts_nop 0\n\tglobal_load_lds_dwordx4 %1, %2\n\ts_mov_b32 m0, %0"
               : "=&s"(keep) : "v"(voff), "s"(sbase), "s"(lds_addr) : "memory");
}
DI unsigned pk2(float a, float b) {
  bf16x2_t r = __builtin_convertvector((f32x2){a, b}, bf16x2_t);
  return __builtin_bit_cast(unsigned, r);
}
DI float bf2f(unsigned short u) { return __uint_as_float(((unsigned)u) << 16); }
typedef short v4i16_t __attribute__((ext_vector_type(4)));
DI s16x4 ldtr(LAS const char* p) { return __builtin_bit_cast(s16x4, __builtin_amdgcn_ds_read_tr16_b64_v4i16((LAS v4i16_t*)p)); }
DI bf16x8 cat(s16x4 lo, s16x4 hi) { return __builtin_shufflevector(lo, hi, 0, 1, 2, 3, 4, 5, 6, 7); }
#define MFMA16(a, b, c) __builtin_amdgcn_mfma_f32_16x16x32_bf16(a, b, c, 0, 0, 0)
#define MFMA32(a, b, c) __builtin_amdgcn_mfma_f32_32x32x16_bf16(a, b, c, 0, 0, 0)

constexpr int K1_XB = 16384, K1_WB = 28672, K1_SLOT = K1_XB + K1_WB;
constexpr int K1_LDS = 3 * K1_SLOT;

template <bool VT>
DI void k1_body(LAS char* lds, const char* xg, const char* wg, int ldw2  , int kind  , int i, int cgl  , char* ws) {
  const int tid = threadIdx.x, lane = tid & 63;
  const int w = __builtin_amdgcn_readfirstlane(tid >> 6);
  const int wm = w >> 1, wn = w & 1;
  const unsigned lds0 = (unsigned)(size_t)lds;
  unsigned xo[4], wo_[7];
#pragma unroll
  for (int tt = 0; tt < 4; ++tt) xo[tt] = (32 * w + 8 * tt + (lane >> 3)) * 896 + 16 * ((lane & 7) ^ ((lane >> 4) | (4 * (tt & 1))));
#pragma unroll
  for (int tt = 0; tt < 7; ++tt) { const int q = (7 * w + tt) * 64 + lane, rho = q / 28, cp = q % 28; wo_[tt] = rho * ldw2 + 16 * (cp ^ (2 * ((rho >> 3) & 1))); }
  auto stage = [&](int kc, int slot) {
    const char* xs = xg + kc * 128; const char* wsrc = wg + (size_t)kc * 64 * ldw2;
    const unsigned lb = lds0 + slot * K1_SLOT;
#pragma unroll
    for (int tt = 0; tt < 4; ++tt) glds16(xs, xo[tt], lb + (4 * w + tt) * 1024);
#pragma unroll
    for (int tt = 0; tt < 7; ++tt) glds16(wsrc, wo_[tt], lb + K1_XB + (7 * w + tt) * 1024);
  };
  stage(0, 0); stage(1, 1); stage(2, 2);
  const int m = lane & 15, g = lane >> 4, q4 = (lane & 15) >> 2, p4 = lane & 3;
  const unsigned xb0 = (64 * wm + m) * 128 + 16 * ((g) ^ (m >> 1));
  const unsigned xb1 = (64 * wm + m) * 128 + 16 * ((4 + g) ^ (m >> 1));
  const unsigned wbb = K1_XB + (8 * g + q4) * 448 + 8 * p4 + 7 * wn * 32;
  const unsigned wbE = wbb + 32 * (g & 1), wbO = wbb - 32 * (g & 1);
  f32x4 acc[4][7];
#pragma unroll
  for (int a = 0; a < 4; ++a)
#pragma unroll
    for (int t = 0; t < 7; ++t) acc[a][t] = (f32x4){0.f, 0.f, 0.f, 0.f};
  bf16x8 xf[2][4], wf[2][7];
  auto ldfrag = [&](auto BUF, auto SLOT, auto KK) {
    constexpr int buf = BUF, slot = SLOT, kk = KK;
    LAS const char* base = lds + slot * K1_SLOT;
#pragma unroll
    for (int a = 0; a < 4; ++a) xf[buf][a] = *(LAS const bf16x8*)(base + (kk ? xb1 : xb0) + a * 2048);
#pragma unroll
    for (int tl = 0; tl < 7; ++tl) {
      const unsigned b = (((wn + tl) & 1) ? wbO : wbE) + tl * 32 + kk * 32 * 448;
      wf[buf][tl] = cat(ldtr(base + b), ldtr(base + b + 4 * 448));
    }
  };
  auto mma = [&](auto BUF) {
    constexpr int buf = BUF;
#pragma unroll
    for (int tl = 0; tl < 7; ++tl)
#pragma unroll
      for (int a = 0; a < 4; ++a) acc[a][tl] = VT ? MFMA16(xf[buf][a], wf[buf][tl], acc[a][tl]) : MFMA16(wf[buf][tl], xf[buf][a], acc[a][tl]);
  };
  using I0 = std::integral_constant<int, 0>; using I1 = std::integral_constant<int, 1>;
  wait_vm<22>(); raw_barrier();
  ldfrag(I0{}, I0{}, I0{});
  static_for<0, 7>([&](auto C) {
    constexpr int c = C, slot = c % 3, nslot = (c + 1) % 3;
    ldfrag(I1{}, std::integral_constant<int, slot>{}, I1{});
    mma(I0{});
    if constexpr (c < 6) {
      wait_lgkm0();
      if constexpr (c + 2 <= 6) wait_vm<(c == 0) ? 11 : 11>(); else wait_vm<0>();
      raw_barrier();
      if constexpr (c + 3 <= 6) stage(c + 3, slot);
      ldfrag(I0{}, std::integral_constant<int, nslot>{}, I0{});
    }
    mma(I1{});
  });
  const int tb0 = 4 * i + 2 * wm;
  if constexpr (!VT) {
    char* dst; int ntile; float sc = 1.f;
    if (kind == 0) { dst = ws + WS_Q; ntile = 28; sc = 0.18898223650461362f * 1.4426950408889634f; }
    else if (kind == 1) { dst = ws + WS_K; ntile = 28; }
    else { dst = ws + WS_H; ntile = 112; }
    const int T0 = cgl * 14 + 7 * wn;
    const bool relu = kind == 3;
#pragma unroll
    for (int pr = 0; pr < 2; ++pr)
#pragma unroll
      for (int tl = 0; tl < 7; ++tl) {
        f32x4 X = acc[2 * pr][tl] * sc, Y = acc[2 * pr + 1][tl] * sc;
        if (relu) { for (int e = 0; e < 4; ++e) { X[e] = fmaxf(X[e], 0.f); Y[e] = fmaxf(Y[e], 0.f); } }
        unsigned x0 = pk2(X[0], X[1]), x1 = pk2(X[2], X[3]), y0 = pk2(Y[0], Y[1]), y1 = pk2(Y[2], Y[3]);
        auto r0 = __builtin_amdgcn_permlane16_swap(x0, y0, false, false);
        auto r1 = __builtin_amdgcn_permlane16_swap(x1, y1, false, false);
        u32x4 v = {r0[0], r1[0], r0[1], r1[1]};
        *(u32x4*)(dst + ((size_t)(tb0 + pr) * ntile + T0 + tl) * 1024 + lane * 16) = v;
      }
  } else {
    char* dst = ws + WS_V;
    const int col0 = cgl * 224 + 112 * wn + (lane & 15);
    const int row = lane >> 4;
#pragma unroll
    for (int pr = 0; pr < 2; ++pr)
#pragma unroll
      for (int tl = 0; tl < 7; ++tl) {
        f32x4 X = acc[2 * pr][tl], Y = acc[2 * pr + 1][tl];
        unsigned x0 = pk2(X[0], X[1]), x1 = pk2(X[2], X[3]), y0 = pk2(Y[0], Y[1]), y1 = pk2(Y[2], Y[3]);
        auto r0 = __builtin_amdgcn_permlane16_swap(x0, y0, false, false);
        auto r1 = __builtin_amdgcn_permlane16_swap(x1, y1, false, false);
        u32x4 v = {r0[0], r1[0], r0[1], r1[1]};
        const int sigma = 2 * (tb0 + pr) + (row & 1), hh = row >> 1;
        *(u32x4*)(dst + ((size_t)(sigma * 2 + hh) * 448 + col0 + 16 * tl) * 16) = v;
      }
  }
}

__global__ __launch_bounds__(256, 1) void k1_proj(const bf16* __restrict__ x, const bf16* __restrict__ wqkv, const bf16* __restrict__ w1,
                                                  const bf16* __restrict__ gw, char* __restrict__ ws) {
  __shared__ __attribute__((aligned(16))) char smem[K1_LDS];
  const int b = blockIdx.x, xcd = b & 7, idx = b >> 3, r = xcd >> 1, c2 = xcd & 1;
  if (idx < 28) {
    const int i = 4 * r + idx / 7, cc = idx % 7;
    const char* xg = (const char*)x + (size_t)i * 128 * 896;
    LAS char* lds = (LAS char*)smem;
    if (cc < 3) {
      const int c = 2 * cc + c2;
      const char* wg = (const char*)wqkv + c * 448;
      if (cc == 2) k1_body<true>(lds, xg, wg, NQKV * 2, 2, i, c2, ws);
      else k1_body<false>(lds, xg, wg, NQKV * 2, cc, i, c2, ws);
    } else {
      const int cf = 4 * c2 + (cc - 3);
      k1_body<false>(lds, xg, (const char*)w1 + cf * 448, DFF * 2, 3, i, cf, ws);
    }
  } else {
    const int gi = (idx - 28) + 4 * xcd, tid = threadIdx.x, lane = tid & 63, w = tid >> 6;
    if (tid < 8) ((unsigned*)(ws + WS_FLAG))[(gi * 8 + tid) * 64] = 0u;
    const int row = gi * 64 + w * 16 + (lane >> 2), part = lane & 3;
    float s = 0.f;
#pragma unroll
    for (int k = 0; k < 14; ++k) {
      const u32x4 xv = *(const u32x4*)((const char*)x + (size_t)row * 896 + k * 64 + part * 16);
      const u32x4 gv = *(const u32x4*)((const char*)gw + k * 64 + part * 16);
#pragma unroll
      for (int e = 0; e < 4; ++e) {
        s += __uint_as_float(xv[e] << 16) * __uint_as_float(gv[e] << 16);
        s += __uint_as_float(xv[e] & 0xffff0000u) * __uint_as_float(gv[e] & 0xffff0000u);
      }
    }
    s += __shfl_xor(s, 1); s += __shfl_xor(s, 2);
    if (part == 0) ((float*)(ws + WS_G))[row] = 1.f / (1.f + __expf(-s));
  }
}

constexpr int K2_D = 6, K2_SLOT = 4096, K2_RING = K2_D * K2_SLOT;
constexpr int K2_K = 4 * K2_RING, K2_V = K2_K + 12 * 2048, K2_CNT = K2_V + 12 * 2048, K2_LDS = K2_CNT + 16;
constexpr int RED_STRIDE = 272;

DI void k2_attention(LAS char* lds, char* ws, int i, int h, int w4, int lane) {
  const unsigned lds0 = (unsigned)(size_t)lds;
  const int u0 = (h & 1) ? (28 * h - 4) / 8 : (28 * h) / 8;
  const int hh = lane >> 5, ql = lane & 31;
  {
    const unsigned kvo = lane * 16, vvo = (hh * 448 + 28 * h + ql) * 16;
#pragma unroll
    for (int bq = 0; bq < 3; ++bq) {
      const int bb = 3 * w4 + bq; int tb = 4 * i - 8 + bb; tb = tb < 0 ? 0 : tb;
#pragma unroll
      for (int s = 0; s < 2; ++s) {
        glds16(ws + WS_K + (size_t)tb * FRAG_TB + (u0 + 2 * s) * 512, kvo, lds0 + K2_K + bb * 2048 + s * 1024);
        glds16(ws + WS_V + (size_t)(2 * tb + s) * 2 * 448 * 16, vvo, lds0 + K2_V + bb * 2048 + s * 1024);
      }
    }
  }
  const int tbq = 4 * i + w4;
  u32x4 q0 = *(const u32x4*)(ws + WS_Q + (size_t)tbq * FRAG_TB + u0 * 512 + lane * 16);
  u32x4 q1 = *(const u32x4*)(ws + WS_Q + (size_t)tbq * FRAG_TB + (u0 + 2) * 512 + lane * 16);
  const float gq = ((const float*)(ws + WS_G))[tbq * 32 + ql];
  if (h & 1) { if (hh == 0) { q0[0] = 0; q0[1] = 0; } } else { if (hh == 1) { q1[2] = 0; q1[3] = 0; } }
  wait_vm<0>();
  if (ql >= 28) {
    const u32x4 ones = {0x3f803f80u, 0x3f803f80u, 0x3f803f80u, 0x3f803f80u};
#pragma unroll
    for (int bq = 0; bq < 3; ++bq)
#pragma unroll
      for (int s = 0; s < 2; ++s) *(LAS u32x4*)(lds + K2_V + (3 * w4 + bq) * 2048 + s * 1024 + lane * 16) = ones;
  }
  wait_lgkm0();
  raw_barrier();
  const bf16x8 qa = __builtin_bit_cast(bf16x8, q0), qb = __builtin_bit_cast(bf16x8, q1);
  const int sw = (ql & 0x13) | ((ql & 4) << 1) | ((ql & 8) >> 1);
  LAS const char* kp = lds + K2_K + w4 * 2048 + hh * 512 + sw * 16;
  LAS const char* vp = lds + K2_V + w4 * 2048 + lane * 16;
  f32x16 sc[9];
  const f32x16 zero = {};
#pragma unroll
  for (int j = 0; j < 9; ++j) {
    const bf16x8 k0 = *(LAS const bf16x8*)(kp + j * 2048), k1 = *(LAS const bf16x8*)(kp + j * 2048 + 1024);
    sc[j] = MFMA32(k0, qa, zero); sc[j] = MFMA32(k1, qb, sc[j]);
  }
  const float NEG = -1e30f;
#pragma unroll
  for (int r = 0; r < 16; ++r) {
    const int kap = (r & 7) + 16 * (r >> 3) + 8 * hh;
    sc[0][r] = (kap > ql) ? sc[0][r] : NEG;
    sc[8][r] = (kap <= ql) ? sc[8][r] : NEG;
  }
  if (tbq < 8) {
#pragma unroll
    for (int j = 0; j < 8; ++j)
      if (tbq - 8 + j < 0) {
#pragma unroll
        for (int r = 0; r < 16; ++r) sc[j][r] = NEG;
      }
  }
  float mx = NEG;
#pragma unroll
  for (int j = 0; j < 9; ++j)
#pragma unroll
    for (int r = 0; r < 16; r += 2) mx = fmaxf(fmaxf(mx, sc[j][r]), sc[j][r + 1]);
  { auto rr = __builtin_amdgcn_permlane32_swap(__float_as_uint(mx), __float_as_uint(mx), false, false);
    mx = fmaxf(__uint_as_float(rr[0]), __uint_as_float(rr[1])); }
  f32x16 o = zero;
#pragma unroll
  for (int j = 0; j < 9; ++j) {
    unsigned pw[8];
#pragma unroll
    for (int r = 0; r < 16; r += 2)
      pw[r >> 1] = pk2(__builtin_amdgcn_exp2f(sc[j][r] - mx), __builtin_amdgcn_exp2f(sc[j][r + 1] - mx));
    const bf16x8 p0 = __builtin_bit_cast(bf16x8, (u32x4){pw[0], pw[1], pw[2], pw[3]});
    const bf16x8 p1 = __builtin_bit_cast(bf16x8, (u32x4){pw[4], pw[5], pw[6], pw[7]});
    const bf16x8 v0 = *(LAS const bf16x8*)(vp + j * 2048), v1 = *(LAS const bf16x8*)(vp + j * 2048 + 1024);
    o = MFMA32(v0, p0, o); o = MFMA32(v1, p1, o);
  }
  float l;
  { auto rr = __builtin_amdgcn_permlane32_swap(__float_as_uint(o[12]), __float_as_uint(o[12]), false, false);
    l = hh ? o[12] : __uint_as_float(rr[1]); }
  const float scl = gq / l;
  u32x4 f0, f1;
  f0[0] = pk2(o[0] * scl, o[1] * scl); f0[1] = pk2(o[2] * scl, o[3] * scl); f0[2] = pk2(o[4] * scl, o[5] * scl); f0[3] = pk2(o[6] * scl, o[7] * scl);
  f1[0] = pk2(o[8] * scl, o[9] * scl); f1[1] = pk2(o[10] * scl, o[11] * scl); f1[2] = pk2(o[12] * scl, o[13] * scl); f1[3] = pk2(o[14] * scl, o[15] * scl);
  if (hh) { f1[2] = 0; f1[3] = 0; }
  {
    const auto rs = __builtin_amdgcn_make_buffer_rsrc((void*)(ws + WS_O), 0, 64 * 16 * 2048, 0x00020000);
    const unsigned off = (tbq * 16 + h) * 2048 + lane * 16;
    __builtin_amdgcn_raw_buffer_store_b128(f0, rs, off, 0, 16);
    __builtin_amdgcn_raw_buffer_store_b128(f1, rs, off + 1024, 0, 16);
  }
  wait_vm<0>();
  unsigned old = 0;
  if (lane == 0) old = __hip_atomic_fetch_add((LAS unsigned*)(lds + K2_CNT), 1u, __ATOMIC_RELAXED, __HIP_MEMORY_SCOPE_WORKGROUP);
  old = __builtin_amdgcn_readfirstlane(old);
  if (old == 3 && lane == 0)
    __hip_atomic_store((GAS unsigned*)(ws + WS_FLAG) + (i * 16 + h) * 64, 1u, __ATOMIC_RELAXED, __HIP_MEMORY_SCOPE_AGENT);
}

DI void k2_gemm(LAS char* lds, const char* w2, const char* wo, char* ws, int rt, int ctile, int w, int lane, f32x16 (&acc)[2][2]) {
  const unsigned ring0 = (unsigned)(size_t)lds + w * K2_RING;
  LAS char* ring = lds + w * K2_RING;
  const int tb0 = 2 * rt;
  const char* hsrc = ws + WS_H + ((size_t)tb0 * 112 + 28 * w) * 1024;
  const char* wsrc = w2 + (size_t)(448 * w) * 896 + ctile * 128;
  const char* osrc = wo + ctile * 128;
  const unsigned hv = lane * 16;
  const unsigned chs = 16 * ((lane & 7) ^ (4 * ((lane >> 4) & 1)));
  const unsigned wv = (lane >> 3) * 896 + chs;
  const int orow = ((lane >> 3) & 3) + 8 * (lane >> 5);
  auto issue = [&](auto S) {
    constexpr int s = S, slot = s % K2_D;
    const unsigned lb = ring0 + slot * K2_SLOT;
    if constexpr (s < 28) {
      glds16(hsrc + s * 1024, hv, lb);
      glds16(hsrc + s * 1024 + 112 * 1024, hv, lb + 1024);
      glds16(wsrc + (size_t)(16 * s) * 896, wv, lb + 2048);
      glds16(wsrc + (size_t)(16 * s + 8) * 896, wv, lb + 3072);
    } else if constexpr (s < 34) {
      auto one = [&](int o, unsigned dst) {
        const int rb = 28 * (4 * w + (o >> 1)) + 16 * (o & 1);
        const int ra = min(rb + orow, 447), rc = min(rb + orow + 4, 447);
        glds16(osrc, ra * 896 + chs, dst);
        glds16(osrc, rc * 896 + chs, dst + 1024);
      };
      one(s - 28, lb + 2048);
      if constexpr (s >= 32) one(s - 28 + 2, lb);
    }
  };
  const int hh = lane >> 5, blk = (lane >> 4) & 1, q4 = (lane & 15) >> 2, p4 = lane & 3;
  const unsigned tb_0 = 2048 + (8 * hh + q4) * 128 + 16 * ((0 + 2 * blk + (p4 >> 1)) ^ (4 * ((q4 >> 1) & 1))) + 8 * (p4 & 1);
  const unsigned tb_1 = 2048 + (8 * hh + q4) * 128 + 16 * ((4 + 2 * blk + (p4 >> 1)) ^ (4 * ((q4 >> 1) & 1))) + 8 * (p4 & 1);
  bf16x8 hf[2][2], wf[2][2];
  auto ldw = [&](auto S) {
    constexpr int s = S, slot = (s < 34 ? s : s - 2) % K2_D, buf = s & 1;
    LAS const char* b = ring + slot * K2_SLOT - (s < 34 ? 0 : 2048);
    wf[buf][0] = cat(ldtr(b + tb_0), ldtr(b + tb_0 + 512));
    wf[buf][1] = cat(ldtr(b + tb_1), ldtr(b + tb_1 + 512));
    if constexpr (s < 28) {
      hf[buf][0] = *(LAS const bf16x8*)(b + lane * 16);
      hf[buf][1] = *(LAS const bf16x8*)(b + 1024 + lane * 16);
    }
  };
  static_for<0, K2_D>([&](auto S) { issue(S); });
  const float g0 = ((const float*)(ws + WS_G))[tb0 * 32 + (lane & 31)], g1 = ((const float*)(ws + WS_G))[tb0 * 32 + 32 + (lane & 31)];
  raw_barrier();
  wait_vm<0>();
  ldw(std::integral_constant<int, 0>{});
  static_for<0, 28>([&](auto S) {
    constexpr int s = S, buf = s & 1;
    if constexpr (s + 1 < 36) {
      constexpr int allow = [] { int n = 0; for (int t = s + 2; t <= s + K2_D - 1; ++t) n += (t < 28 ? 4 : (t < 32 ? 2 : (t < 34 ? 4 : 0))); return n; }();
      wait_vm<allow>();
    }
    if constexpr (s + 1 < 28) ldw(std::integral_constant<int, s + 1>{});
#pragma unroll
    for (int ct = 0; ct < 2; ++ct)
#pragma unroll
      for (int tt = 0; tt < 2; ++tt) acc[ct][tt] = MFMA32(wf[buf][ct], hf[buf][tt], acc[ct][tt]);
    wait_lgkm0();
    issue(std::integral_constant<int, s + K2_D>{});
  });
  const float s0 = 1.f - g0, s1 = 1.f - g1;
#pragma unroll
  for (int ct = 0; ct < 2; ++ct) { acc[ct][0] *= s0; acc[ct][1] *= s1; }
  const int i = rt >> 1;
  {
    GAS unsigned* fl = (GAS unsigned*)(ws + WS_FLAG) + (i * 16 + 4 * w + (lane & 3)) * 64;
    for (unsigned spins = 0; spins < (1u << 22); ++spins) {
      const unsigned v = __hip_atomic_load(fl, __ATOMIC_RELAXED, __HIP_MEMORY_SCOPE_AGENT);
      if (__all(v == 1u)) break;
      __builtin_amdgcn_s_sleep(1);
    }
    asm volatile("" ::: "memory");
  }
  u32x4 of[8][2];
  {
    const auto rs = __builtin_amdgcn_make_buffer_rsrc((void*)(ws + WS_O), 0, 64 * 16 * 2048, 0x00020000);
#pragma unroll
    for (int o = 0; o < 8; ++o)
#pragma unroll
      for (int tt = 0; tt < 2; ++tt)
        of[o][tt] = __builtin_amdgcn_raw_buffer_load_b128(rs, ((tb0 + tt) * 16 + 4 * w + (o >> 1)) * 2048 + (o & 1) * 1024 + lane * 16, 0, 16);
  }
  wait_vm<0>();
  static_for<28, 36>([&](auto S) {
    constexpr int s = S, buf = s & 1, o = s - 28;
    ldw(S);
#pragma unroll
    for (int ct = 0; ct < 2; ++ct)
#pragma unroll
      for (int tt = 0; tt < 2; ++tt) acc[ct][tt] = MFMA32(wf[buf][ct], __builtin_bit_cast(bf16x8, of[o][tt]), acc[ct][tt]);
  });
}

__global__ __launch_bounds__(512, 2) void k2_attn_out(const bf16* __restrict__ w2, const bf16* __restrict__ wo, char* __restrict__ ws, bf16* __restrict__ out) {
  __shared__ __attribute__((aligned(16))) char smem[K2_LDS];
  LAS char* lds = (LAS char*)smem;
  const int b = blockIdx.x, xcd = b & 7, idx = b >> 3, r = xcd >> 1, c2 = xcd & 1;
  const int tid = threadIdx.x, lane = tid & 63;
  const int w = __builtin_amdgcn_readfirstlane(tid >> 6);
  const bool has_gemm = idx < 28;
  const int rt = 8 * r + 4 * c2 + idx / 7, ctile = idx % 7;
  if (tid == 256) *(LAS unsigned*)(lds + K2_CNT) = 0u;
  if (w >= 4) {
    k2_attention(lds, ws, 4 * r + (idx >> 3), 8 * c2 + (idx & 7), w - 4, lane);
  } else if (has_gemm) {
    f32x16 acc[2][2] = {};
    k2_gemm(lds, (const char*)w2, (const char*)wo, ws, rt, ctile, w, lane, acc);
    wait_lgkm0();
    LAS char* red = lds + w * K2_RING;
    const int hh = lane >> 5;
#pragma unroll
    for (int ct = 0; ct < 2; ++ct)
#pragma unroll
      for (int tt = 0; tt < 2; ++tt)
#pragma unroll
        for (int rg = 0; rg < 4; ++rg) {
          const f32x4 v = {acc[ct][tt][4 * rg], acc[ct][tt][4 * rg + 1], acc[ct][tt][4 * rg + 2], acc[ct][tt][4 * rg + 3]};
          *(LAS f32x4*)(red + (32 * tt + (lane & 31)) * RED_STRIDE + (32 * ct + 8 * rg + 4 * hh) * 4) = v;
        }
  } else {
    raw_barrier();
  }
  if (!has_gemm) return;
  __syncthreads();
  {
    const int tok = tid >> 3, cg = tid & 7;
    f32x4 a = {0, 0, 0, 0}, c = {0, 0, 0, 0};
#pragma unroll
    for (int ww = 0; ww < 4; ++ww) {
      a += *(LAS const f32x4*)(lds + ww * K2_RING + tok * RED_STRIDE + cg * 32);
      c += *(LAS const f32x4*)(lds + ww * K2_RING + tok * RED_STRIDE + cg * 32 + 16);
    }
    const u32x4 v = {pk2(a[0], a[1]), pk2(a[2], a[3]), pk2(c[0], c[1]), pk2(c[2], c[3])};
    *(u32x4*)((char*)out + (size_t)(rt * 64 + tok) * 896 + ctile * 128 + cg * 16) = v;
  }
}

extern "C" void kernel_launch(void* const* d_in, const int* in_sizes, int n_in, void* d_out, int out_size, void* d_ws, size_t ws_size, hipStream_t stream) {
  const bf16* x = (const bf16*)d_in[0]; const bf16* wqkv = (const bf16*)d_in[1]; const bf16* wo = (const bf16*)d_in[2];
  const bf16* w1 = (const bf16*)d_in[4]; const bf16* w2 = (const bf16*)d_in[5]; const bf16* gw = (const bf16*)d_in[6];
  k1_proj<<<256, 256, 0, stream>>>(x, wqkv, w1, gw, (char*)d_ws);
  k2_attn_out<<<256, 512, 0, stream>>>(w2, wo, (char*)d_ws, (bf16*)d_out);
}
```

```cpp
#include <hip/hip_runtime.h>
#include <hip/hip_bf16.h>
#include <utility>
#include <type_traits>

typedef __hip_bfloat16 bf16;
typedef short bf16x8 __attribute__((ext_vector_type(8)));
typedef short s16x4 __attribute__((ext_vector_type(4)));
typedef float f32x4 __attribute__((ext_vector_type(4)));
typedef float f32x2 __attribute__((ext_vector_type(2)));
typedef float f32x16 __attribute__((ext_vector_type(16)));
typedef unsigned u32x4 __attribute__((ext_vector_type(4)));
typedef unsigned u32x2 __attribute__((ext_vector_type(2)));
typedef __bf16 bf16x2_t __attribute__((ext_vector_type(2)));
#define LAS __attribute__((address_space(3)))
#define GAS __attribute__((address_space(1)))
#define DI __device__ __forceinline__

constexpr int S_ = 2048, D_ = 448, NQKV = 1344, DFF = 1792;
constexpr size_t WS_FLAG = 0;
constexpr size_t WS_G    = 65536;
constexpr size_t WS_Q    = 131072;
constexpr size_t FRAG_TB = 28 * 1024;
constexpr size_t WS_K    = WS_Q + 64 * FRAG_TB;
constexpr size_t WS_V    = WS_K + 64 * FRAG_TB;
constexpr size_t WS_H    = WS_V + 64 * FRAG_TB + 4096;
constexpr size_t WS_O    = WS_H + 64 * 112 * 1024;

#define STAMPW(k, j) do {} while (0)
template <int I, int N, class F> DI void static_for(F&& f) {
  if constexpr (I < N) { f(std::integral_constant<int, I>{}); static_for<I + 1, N>(f); }
}
template <int N> DI void wait_vm() { asm volatile("s_waitcnt vmcnt(%0)" ::"n"(N) : "memory"); }
DI void wait_lgkm0() { asm volatile("s_waitcnt lgkmcnt(0)" ::: "memory"); }
DI void raw_barrier() { asm volatile("s_barrier" ::: "memory"); }
DI void glds16(const void* sbase, unsigned voff, unsigned lds_addr) {
  unsigned keep;
  asm volatile("s_mov_b32 %0, m0\n\ts_mov_b32 m0, %3\n\ts_nop 0\n\tglobal_load_lds_dwordx4 %1, %2\n\ts_mov_b32 m0, %0"
               : "=&s"(keep) : "v"(voff), "s"(sbase), "s"(lds_addr) : "memory");
}
DI unsigned pk2(float a, float b) {
  bf16x2_t r = __builtin_convertvector((f32x2){a, b}, bf16x2_t);
  return __builtin_bit_cast(unsigned, r);
}
DI float bf2f(unsigned short u) { return __uint_as_float(((unsigned)u) << 16); }
typedef short v4i16_t __attribute__((ext_vector_type(4)));
DI s16x4 ldtr(LAS const char* p) { return __builtin_bit_cast(s16x4, __builtin_amdgcn_ds_read_tr16_b64_v4i16((LAS v4i16_t*)p)); }
DI bf16x8 cat(s16x4 lo, s16x4 hi) { return __builtin_shufflevector(lo, hi, 0, 1, 2, 3, 4, 5, 6, 7); }
#define MFMA16(a, b, c) __builtin_amdgcn_mfma_f32_16x16x32_bf16(a, b, c, 0, 0, 0)
#define MFMA32(a, b, c) __builtin_amdgcn_mfma_f32_32x32x16_bf16(a, b, c, 0, 0, 0)

constexpr int K1_XB = 16384, K1_WB = 28672, K1_SLOT = K1_XB + K1_WB;
constexpr int K1_LDS = 3 * K1_SLOT;

#define SB() __builtin_amdgcn_sched_barrier(0)
typedef short s16x2 __attribute__((ext_vector_type(2)));
DI unsigned relu2(unsigned x) { s16x2 v = __builtin_bit_cast(s16x2, x); v = __builtin_elementwise_max(v, (s16x2){0, 0}); return __builtin_bit_cast(unsigned, v); }

DI void k1_loader(LAS char* lds, const char* xg, const char* wg, int ldw2, int w, char* ws) {
  const int lane = threadIdx.x & 63;
  const unsigned lds0 = (unsigned)(size_t)lds;
  unsigned xo[4], wo_[7];
#pragma unroll
  for (int tt = 0; tt < 4; ++tt) xo[tt] = (32 * w + 8 * tt + (lane >> 3)) * 896 + 16 * ((lane & 7) ^ ((lane >> 4) | (4 * (tt & 1))));
#pragma unroll
  for (int tt = 0; tt < 7; ++tt) { const int q = (7 * w + tt) * 64 + lane, rho = q / 28, cp = q % 28; wo_[tt] = rho * ldw2 + 16 * (cp ^ (2 * ((rho >> 3) & 1))); }
  auto stage = [&](int kc, int slot) {
    const char* xs = xg + kc * 128; const char* wsrc = wg + (size_t)kc * 64 * ldw2;
    const unsigned lb = lds0 + slot * K1_SLOT;
#pragma unroll
    for (int tt = 0; tt < 4; ++tt) glds16(xs, xo[tt], lb + (4 * w + tt) * 1024);
#pragma unroll
    for (int tt = 0; tt < 7; ++tt) glds16(wsrc, wo_[tt], lb + K1_XB + (7 * w + tt) * 1024);
  };
  STAMPW(0, 0);
  stage(0, 0); stage(1, 1);
  wait_vm<11>(); raw_barrier();
  STAMPW(0, 1);
  stage(2, 2);
  static_for<0, 6>([&](auto C) {
    constexpr int c = C;
    if constexpr (c + 2 <= 6) wait_vm<11>(); else wait_vm<0>();
    raw_barrier();
    if constexpr (c + 3 <= 6) stage(c + 3, c % 3);
  });
  STAMPW(0, 2);
}

template <bool VT>
DI void k1_compute(LAS char* lds, int kind  , int i, int cgl, int w, char* ws) {
  const int lane = threadIdx.x & 63;
  const int wm = w >> 1, wn = w & 1;
  const int m = lane & 15, g = lane >> 4, q4 = (lane & 15) >> 2, p4 = lane & 3;
  const unsigned xb0 = (64 * wm + m) * 128 + 16 * ((g) ^ (m >> 1));
  const unsigned xb1 = (64 * wm + m) * 128 + 16 * ((4 + g) ^ (m >> 1));
  const unsigned wbb = K1_XB + (8 * g + q4) * 448 + 8 * p4 + 7 * wn * 32;
  const unsigned wE = wbb + ((wn & 1) ? -32 * (g & 1) : 32 * (g & 1));
  const unsigned wO = wbb + ((wn & 1) ? 32 * (g & 1) : -32 * (g & 1));
  f32x4 acc[4][7];
#pragma unroll
  for (int a = 0; a < 4; ++a)
#pragma unroll
    for (int t = 0; t < 7; ++t) acc[a][t] = (f32x4){0.f, 0.f, 0.f, 0.f};
  bf16x8 xf[2][4]; s16x4 wl[2][7], wh[2][7];
  auto rd = [&](auto BUF, auto SLOT, auto KK, auto R) {
    constexpr int buf = BUF, slot = SLOT, kk = KK, r = R;
    LAS const char* base = lds + slot * K1_SLOT;
    if constexpr (r < 4) xf[buf][r] = *(LAS const bf16x8*)(base + (kk ? xb1 : xb0) + r * 2048);
    else {
      constexpr int tl = (r - 4) >> 1, hi = (r - 4) & 1;
      const unsigned b = ((tl & 1) ? wO : wE) + tl * 32 + kk * 32 * 448 + hi * 4 * 448;
      if constexpr (hi) wh[buf][tl] = ldtr(base + b); else wl[buf][tl] = ldtr(base + b);
    }
  };
  auto mm = [&](auto BUF, auto J) {
    constexpr int buf = BUF, j = J, tl = j >> 2, a = j & 3;
    const bf16x8 wf = cat(wl[buf][tl], wh[buf][tl]);
    acc[a][tl] = VT ? MFMA16(xf[buf][a], wf, acc[a][tl]) : MFMA16(wf, xf[buf][a], acc[a][tl]);
  };
  using I0 = std::integral_constant<int, 0>; using I1 = std::integral_constant<int, 1>;
  STAMPW(0, 0);
  raw_barrier();
  STAMPW(0, 1);
  static_for<0, 18>([&](auto R) { rd(I0{}, I0{}, I0{}, R); });
  SB();
  static_for<0, 7>([&](auto C) {
    constexpr int c = C, slot = c % 3, nslot = (c + 1) % 3;
    static_for<0, 28>([&](auto J) {
      mm(I0{}, J);
      if constexpr (J < 18) rd(I1{}, std::integral_constant<int, slot>{}, I1{}, J);
      SB();
    });
    if constexpr (c < 6) {
      wait_lgkm0(); raw_barrier(); SB();
      static_for<0, 28>([&](auto J) {
        mm(I1{}, J);
        if constexpr (J < 18) rd(I0{}, std::integral_constant<int, nslot>{}, I0{}, J);
        SB();
      });
    } else {
      static_for<0, 28>([&](auto J) { mm(I1{}, J); });
    }
  });
  STAMPW(0, 8);
  const int tb0 = 4 * i + 2 * wm;
  if constexpr (!VT) {
    char* dst; int ntile;
    if (kind == 0) { dst = ws + WS_Q; ntile = 28; }
    else if (kind == 1) { dst = ws + WS_K; ntile = 28; }
    else { dst = ws + WS_H; ntile = 112; }
    const int T0 = cgl * 14 + 7 * wn;
    dst += ((size_t)tb0 * ntile + T0) * 1024 + lane * 16;
    if (kind == 0) {
      const float sc = 0.18898223650461362f * 1.4426950408889634f;
#pragma unroll
      for (int tl = 0; tl < 7; ++tl)
#pragma unroll
        for (int a = 0; a < 4; ++a) acc[a][tl] *= sc;
    }
    const bool relu = kind == 3;
#pragma unroll
    for (int tl = 0; tl < 7; ++tl)
#pragma unroll
      for (int pr = 0; pr < 2; ++pr) {
        const f32x4 X = acc[2 * pr][tl], Y = acc[2 * pr + 1][tl];
        unsigned x0 = pk2(X[0], X[1]), x1 = pk2(X[2], X[3]), y0 = pk2(Y[0], Y[1]), y1 = pk2(Y[2], Y[3]);
        if (relu) { x0 = relu2(x0); x1 = relu2(x1); y0 = relu2(y0); y1 = relu2(y1); }
        auto r0 = __builtin_amdgcn_permlane16_swap(x0, y0, false, false);
        auto r1 = __builtin_amdgcn_permlane16_swap(x1, y1, false, false);
        u32x4 v = {r0[0], r1[0], r0[1], r1[1]};
        *(u32x4*)(dst + ((size_t)pr * ntile + tl) * 1024) = v;
      }
  } else {
    const int row = lane >> 4;
    char* dst = ws + WS_V + ((size_t)((2 * tb0 + (row & 1)) * 2 + (row >> 1)) * 448 + cgl * 224 + 112 * wn + (lane & 15)) * 16;
#pragma unroll
    for (int tl = 0; tl < 7; ++tl)
#pragma unroll
      for (int pr = 0; pr < 2; ++pr) {
        const f32x4 X = acc[2 * pr][tl], Y = acc[2 * pr + 1][tl];
        unsigned x0 = pk2(X[0], X[1]), x1 = pk2(X[2], X[3]), y0 = pk2(Y[0], Y[1]), y1 = pk2(Y[2], Y[3]);
        auto r0 = __builtin_amdgcn_permlane16_swap(x0, y0, false, false);
        auto r1 = __builtin_amdgcn_permlane16_swap(x1, y1, false, false);
        u32x4 v = {r0[0], r1[0], r0[1], r1[1]};
        *(u32x4*)(dst + (size_t)(pr * 4 * 448 + 16 * tl) * 16) = v;
      }
  }
  STAMPW(0, 9);
}

__global__ __launch_bounds__(512, 2) void k1_proj(const bf16* __restrict__ x, const bf16* __restrict__ wqkv, const bf16* __restrict__ w1,
                                                  const bf16* __restrict__ gw, char* __restrict__ ws) {
  __shared__ __attribute__((aligned(16))) char smem[K1_LDS];
  const int b = blockIdx.x, xcd = b & 7, idx = b >> 3, r = xcd >> 1, c2 = xcd & 1;
  const int w = __builtin_amdgcn_readfirstlane(threadIdx.x >> 6);
  if (idx < 28) {
    const int i = 4 * r + idx / 7, cc = idx % 7;
    LAS char* lds = (LAS char*)smem;
    if (w >= 4) {
      const char* xg = (const char*)x + (size_t)i * 128 * 896;
      if (cc < 3) k1_loader(lds, xg, (const char*)wqkv + (2 * cc + c2) * 448, NQKV * 2, w - 4, ws);
      else k1_loader(lds, xg, (const char*)w1 + (4 * c2 + cc - 3) * 448, DFF * 2, w - 4, ws);
    } else {
      if (cc == 2) k1_compute<true>(lds, 2, i, c2, w, ws);
      else k1_compute<false>(lds, cc < 3 ? cc : 3, i, cc < 3 ? c2 : 4 * c2 + cc - 3, w, ws);
    }
  } else {
    const int gi = (idx - 28) + 4 * xcd, tid = threadIdx.x, lane = tid & 63;
    if (tid < 8) ((unsigned*)(ws + WS_FLAG))[(gi * 8 + tid) * 64] = 0u;
    if (w < 4) {
      const int row = gi * 64 + w * 16 + (lane >> 2), part = lane & 3;
      float s = 0.f;
#pragma unroll
      for (int k = 0; k < 14; ++k) {
        const u32x4 xv = *(const u32x4*)((const char*)x + (size_t)row * 896 + k * 64 + part * 16);
        const u32x4 gv = *(const u32x4*)((const char*)gw + k * 64 + part * 16);
#pragma unroll
        for (int e = 0; e < 4; ++e) {
          s += __uint_as_float(xv[e] << 16) * __uint_as_float(gv[e] << 16);
          s += __uint_as_float(xv[e] & 0xffff0000u) * __uint_as_float(gv[e] & 0xffff0000u);
        }
      }
      s += __shfl_xor(s, 1); s += __shfl_xor(s, 2);
      if (part == 0) ((float*)(ws + WS_G))[row] = 1.f / (1.f + __expf(-s));
    }
  }
}

#define KD 6
constexpr int K2_D = KD, K2_SLOT = 4096, K2_RING = K2_D * K2_SLOT;
constexpr int K2_K = 4 * K2_RING, K2_V = K2_K + 12 * 2048, K2_CNT = K2_V + 12 * 2048, K2_LDS = K2_CNT + 16;
constexpr int RED_STRIDE = 272;

DI void k2_attention(LAS char* lds, char* ws, int i, int h, int w4, int lane) {
  const unsigned lds0 = (unsigned)(size_t)lds;
  STAMPW(1, 0);
  const int u0 = (h & 1) ? (28 * h - 4) / 8 : (28 * h) / 8;
  const int hh = lane >> 5, ql = lane & 31;
  {
    const unsigned kvo = lane * 16, vvo = (hh * 448 + 28 * h + ql) * 16;
#pragma unroll
    for (int bq = 0; bq < 3; ++bq) {
      const int bb = 3 * w4 + bq; int tb = 4 * i - 8 + bb; tb = tb < 0 ? 0 : tb;
#pragma unroll
      for (int s = 0; s < 2; ++s) {
        glds16(ws + WS_K + (size_t)tb * FRAG_TB + (u0 + 2 * s) * 512, kvo, lds0 + K2_K + bb * 2048 + s * 1024);
        glds16(ws + WS_V + (size_t)(2 * tb + s) * 2 * 448 * 16, vvo, lds0 + K2_V + bb * 2048 + s * 1024);
      }
    }
  }
  const int tbq = 4 * i + w4;
  u32x4 q0 = *(const u32x4*)(ws + WS_Q + (size_t)tbq * FRAG_TB + u0 * 512 + lane * 16);
  u32x4 q1 = *(const u32x4*)(ws + WS_Q + (size_t)tbq * FRAG_TB + (u0 + 2) * 512 + lane * 16);
  const float gq = ((const float*)(ws + WS_G))[tbq * 32 + ql];
  if (h & 1) { if (hh == 0) { q0[0] = 0; q0[1] = 0; } } else { if (hh == 1) { q1[2] = 0; q1[3] = 0; } }
  wait_vm<0>();
  if (ql >= 28) {
    const u32x4 ones = {0x3f803f80u, 0x3f803f80u, 0x3f803f80u, 0x3f803f80u};
#pragma unroll
    for (int bq = 0; bq < 3; ++bq)
#pragma unroll
      for (int s = 0; s < 2; ++s) *(LAS u32x4*)(lds + K2_V + (3 * w4 + bq) * 2048 + s * 1024 + lane * 16) = ones;
  }
  wait_lgkm0();
  STAMPW(1, 1);
  raw_barrier();
  STAMPW(1, 2);
  const bf16x8 qa = __builtin_bit_cast(bf16x8, q0), qb = __builtin_bit_cast(bf16x8, q1);
  const int sw = (ql & 0x13) | ((ql & 4) << 1) | ((ql & 8) >> 1);
  LAS const char* kp = lds + K2_K + w4 * 2048 + hh * 512 + sw * 16;
  LAS const char* vp = lds + K2_V + w4 * 2048 + lane * 16;
  const f32x16 zero = {};
  const float NEG = -1e30f;
  auto mask0 = [&](f32x16& s) {
#pragma unroll
    for (int r = 0; r < 16; ++r) { const int kap = (r & 7) + 16 * (r >> 3) + 8 * hh; s[r] = (kap > ql) ? s[r] : NEG; } };
  auto mask8 = [&](f32x16& s) {
#pragma unroll
    for (int r = 0; r < 16; ++r) { const int kap = (r & 7) + 16 * (r >> 3) + 8 * hh; s[r] = (kap <= ql) ? s[r] : NEG; } };
  auto kfrag = [&](int j, int s) { return *(LAS const bf16x8*)(kp + j * 2048 + s * 1024); };
  auto vfrag = [&](int j, int s) { return *(LAS const bf16x8*)(vp + j * 2048 + s * 1024); };
  auto max16 = [&](const f32x16& s, float& m0, float& m1) {
#pragma unroll
    for (int r = 0; r < 16; r += 4) { m0 = fmaxf(fmaxf(m0, s[r]), s[r + 1]); m1 = fmaxf(fmaxf(m1, s[r + 2]), s[r + 3]); } };
  auto expack = [&](const f32x16& s, bf16x8& p0, bf16x8& p1) {
    unsigned pw[8];
#pragma unroll
    for (int r = 0; r < 16; r += 2) pw[r >> 1] = pk2(__builtin_amdgcn_exp2f(s[r]), __builtin_amdgcn_exp2f(s[r + 1]));
    p0 = __builtin_bit_cast(bf16x8, (u32x4){pw[0], pw[1], pw[2], pw[3]});
    p1 = __builtin_bit_cast(bf16x8, (u32x4){pw[4], pw[5], pw[6], pw[7]}); };
  f32x16 o = zero;
  if (tbq >= 8) {
    float m0 = NEG, m1 = NEG;
    bf16x8 kf[2][2]; f32x16 sv[2];
    kf[0][0] = kfrag(0, 0); kf[0][1] = kfrag(0, 1);
    static_for<0, 10>([&](auto J) {
      constexpr int j = J;
      if constexpr (j + 1 < 9) { kf[(j + 1) & 1][0] = kfrag(j + 1, 0); kf[(j + 1) & 1][1] = kfrag(j + 1, 1); }
      if constexpr (j < 9) { sv[j & 1] = MFMA32(kf[j & 1][0], qa, zero); sv[j & 1] = MFMA32(kf[j & 1][1], qb, sv[j & 1]); }
      if constexpr (j >= 1) {
        if constexpr (j - 1 == 0) mask0(sv[0]);
        if constexpr (j - 1 == 8) mask8(sv[0]);
        max16(sv[(j - 1) & 1], m0, m1);
      }
      SB();
    });
    float mx = fmaxf(m0, m1);
    { auto rr = __builtin_amdgcn_permlane32_swap(__float_as_uint(mx), __float_as_uint(mx), false, false);
      mx = fmaxf(__uint_as_float(rr[0]), __uint_as_float(rr[1])); }
    f32x16 negm;
#pragma unroll
    for (int r = 0; r < 16; ++r) negm[r] = -mx;
    STAMPW(1, 3);
    bf16x8 vf[2];
    kf[0][0] = kfrag(0, 0); kf[0][1] = kfrag(0, 1);
    kf[1][0] = kfrag(1, 0); kf[1][1] = kfrag(1, 1);
    sv[0] = MFMA32(kf[0][0], qa, negm); sv[0] = MFMA32(kf[0][1], qb, sv[0]);
    SB();
    static_for<0, 9>([&](auto J) {
      constexpr int j = J;
      vf[0] = vfrag(j, 0); vf[1] = vfrag(j, 1);
      if constexpr (j + 1 < 9) { sv[(j + 1) & 1] = MFMA32(kf[(j + 1) & 1][0], qa, negm); sv[(j + 1) & 1] = MFMA32(kf[(j + 1) & 1][1], qb, sv[(j + 1) & 1]); }
      if constexpr (j + 2 < 9) { kf[j & 1][0] = kfrag(j + 2, 0); kf[j & 1][1] = kfrag(j + 2, 1); }
      if constexpr (j == 0) mask0(sv[0]);
      if constexpr (j == 8) mask8(sv[0]);
      bf16x8 p0, p1;
      expack(sv[j & 1], p0, p1);
      o = MFMA32(vf[0], p0, o); o = MFMA32(vf[1], p1, o);
      SB();
    });
  } else {
    const int j0 = 8 - tbq;
    float m0 = NEG, m1 = NEG;
    for (int j = j0; j < 9; ++j) {
      f32x16 s = MFMA32(kfrag(j, 0), qa, zero); s = MFMA32(kfrag(j, 1), qb, s);
      if (j == 8) mask8(s);
      max16(s, m0, m1);
    }
    float mx = fmaxf(m0, m1);
    { auto rr = __builtin_amdgcn_permlane32_swap(__float_as_uint(mx), __float_as_uint(mx), false, false);
      mx = fmaxf(__uint_as_float(rr[0]), __uint_as_float(rr[1])); }
    f32x16 negm;
#pragma unroll
    for (int r = 0; r < 16; ++r) negm[r] = -mx;
    for (int j = j0; j < 9; ++j) {
      f32x16 s = MFMA32(kfrag(j, 0), qa, negm); s = MFMA32(kfrag(j, 1), qb, s);
      if (j == 8) mask8(s);
      bf16x8 p0, p1;
      expack(s, p0, p1);
      o = MFMA32(vfrag(j, 0), p0, o); o = MFMA32(vfrag(j, 1), p1, o);
    }
  }
  float l;
  { auto rr = __builtin_amdgcn_permlane32_swap(__float_as_uint(o[12]), __float_as_uint(o[12]), false, false);
    l = hh ? o[12] : __uint_as_float(rr[1]); }
  const float scl = gq / l;
  u32x4 f0, f1;
  f0[0] = pk2(o[0] * scl, o[1] * scl); f0[1] = pk2(o[2] * scl, o[3] * scl); f0[2] = pk2(o[4] * scl, o[5] * scl); f0[3] = pk2(o[6] * scl, o[7] * scl);
  f1[0] = pk2(o[8] * scl, o[9] * scl); f1[1] = pk2(o[10] * scl, o[11] * scl); f1[2] = pk2(o[12] * scl, o[13] * scl); f1[3] = pk2(o[14] * scl, o[15] * scl);
  if (hh) { f1[2] = 0; f1[3] = 0; }
  {
    const auto rs = __builtin_amdgcn_make_buffer_rsrc((void*)(ws + WS_O), 0, 64 * 16 * 2048, 0x00020000);
    const unsigned off = (tbq * 16 + h) * 2048 + lane * 16;
    __builtin_amdgcn_raw_buffer_store_b128(f0, rs, off, 0, 16);
    __builtin_amdgcn_raw_buffer_store_b128(f1, rs, off + 1024, 0, 16);
  }
  STAMPW(1, 4);
  wait_vm<0>();
  STAMPW(1, 5);
  unsigned old = 0;
  if (lane == 0) old = __hip_atomic_fetch_add((LAS unsigned*)(lds + K2_CNT), 1u, __ATOMIC_RELAXED, __HIP_MEMORY_SCOPE_WORKGROUP);
  old = __builtin_amdgcn_readfirstlane(old);
  if (old == 3 && lane == 0)
    __hip_atomic_store((GAS unsigned*)(ws + WS_FLAG) + (i * 16 + h) * 64, 1u, __ATOMIC_RELAXED, __HIP_MEMORY_SCOPE_AGENT);
}

DI void k2_gemm(LAS char* lds, const char* w2, const char* wo, char* ws, int rt, int ctile, int w, int lane, f32x16 (&acc)[2][2]) {
  STAMPW(1, 0);
  const unsigned ring0 = (unsigned)(size_t)lds + w * K2_RING;
  LAS char* ring = lds + w * K2_RING;
  const int tb0 = 2 * rt;
  const char* hsrc = ws + WS_H + ((size_t)tb0 * 112 + 28 * w) * 1024;
  const char* wsrc = w2 + (size_t)(448 * w) * 896 + ctile * 128;
  const char* osrc = wo + ctile * 128;
  const unsigned hv = lane * 16;
  const unsigned chs = 16 * ((lane & 7) ^ (4 * ((lane >> 4) & 1)));
  const unsigned wv = (lane >> 3) * 896 + chs;
  const int orow = ((lane >> 3) & 3) + 8 * (lane >> 5);
  auto issue1 = [&](auto S, auto Dn) {
    constexpr int s = S, d = Dn, slot = s % K2_D;
    const unsigned lb = ring0 + slot * K2_SLOT;
    if constexpr (s < 28) {
      if constexpr (d == 0) glds16(hsrc + s * 1024, hv, lb);
      if constexpr (d == 1) glds16(hsrc + s * 1024 + 112 * 1024, hv, lb + 1024);
      if constexpr (d == 2) glds16(wsrc + (size_t)(16 * s) * 896, wv, lb + 2048);
      if constexpr (d == 3) glds16(wsrc + (size_t)(16 * s + 8) * 896, wv, lb + 3072);
    } else if constexpr (s < 34) {
      if constexpr (d < 2 || s >= 32) {
        constexpr int o = s - 28 + (d >= 2 ? 2 : 0);
        const int rb = 28 * (4 * w + (o >> 1)) + 16 * (o & 1);
        const int ra = min(rb + orow + 4 * (d & 1), 447);
        glds16(osrc, ra * 896 + chs, (d >= 2 ? lb : lb + 2048) + (d & 1) * 1024);
      }
    }
  };
  auto issue = [&](auto S) { static_for<0, 4>([&](auto Dn) { issue1(S, Dn); }); };
  const int hh = lane >> 5, blk = (lane >> 4) & 1, q4 = (lane & 15) >> 2, p4 = lane & 3;
  const unsigned tb_0 = 2048 + (8 * hh + q4) * 128 + 16 * ((0 + 2 * blk + (p4 >> 1)) ^ (4 * ((q4 >> 1) & 1))) + 8 * (p4 & 1);
  const unsigned tb_1 = 2048 + (8 * hh + q4) * 128 + 16 * ((4 + 2 * blk + (p4 >> 1)) ^ (4 * ((q4 >> 1) & 1))) + 8 * (p4 & 1);
  bf16x8 hf[2][2], wf[2][2];
  auto ldw = [&](auto S) {
    constexpr int s = S, slot = (s < 34 ? s : s - 2) % K2_D, buf = s & 1;
    LAS const char* b = ring + slot * K2_SLOT - (s < 34 ? 0 : 2048);
    wf[buf][0] = cat(ldtr(b + tb_0), ldtr(b + tb_0 + 512));
    wf[buf][1] = cat(ldtr(b + tb_1), ldtr(b + tb_1 + 512));
    if constexpr (s < 28) {
      hf[buf][0] = *(LAS const bf16x8*)(b + lane * 16);
      hf[buf][1] = *(LAS const bf16x8*)(b + 1024 + lane * 16);
    }
  };
  static_for<0, K2_D>([&](auto S) { issue(S); });
  const float g0 = ((const float*)(ws + WS_G))[tb0 * 32 + (lane & 31)], g1 = ((const float*)(ws + WS_G))[tb0 * 32 + 32 + (lane & 31)];
  raw_barrier();
  STAMPW(1, 1);
  wait_vm<0>();
  STAMPW(1, 2);
  ldw(std::integral_constant<int, 0>{});
  static_for<0, 28>([&](auto S) {
    constexpr int s = S, buf = s & 1, nb = buf ^ 1;
    wait_lgkm0();
    constexpr int allow = [] { int n = 0; for (int t = s + 2; t <= s + K2_D - 1; ++t) n += (t < 28 ? 4 : (t < 32 ? 2 : (t < 34 ? 4 : 0))); return n; }();
    wait_vm<allow>();
    SB();
    using SN = std::integral_constant<int, s + K2_D>;
    LAS const char* nbp = ring + ((s + 1) % K2_D) * K2_SLOT;
    acc[0][0] = MFMA32(wf[buf][0], hf[buf][0], acc[0][0]);
    if constexpr (s + 1 < 28) wf[nb][0] = cat(ldtr(nbp + tb_0), ldtr(nbp + tb_0 + 512));
    issue1(SN{}, std::integral_constant<int, 0>{}); SB();
    acc[0][1] = MFMA32(wf[buf][0], hf[buf][1], acc[0][1]);
    if constexpr (s + 1 < 28) { hf[nb][0] = *(LAS const bf16x8*)(nbp + lane * 16); hf[nb][1] = *(LAS const bf16x8*)(nbp + 1024 + lane * 16); }
    issue1(SN{}, std::integral_constant<int, 1>{}); SB();
    acc[1][0] = MFMA32(wf[buf][1], hf[buf][0], acc[1][0]);
    if constexpr (s + 1 < 28) wf[nb][1] = cat(ldtr(nbp + tb_1), ldtr(nbp + tb_1 + 512));
    issue1(SN{}, std::integral_constant<int, 2>{}); SB();
    acc[1][1] = MFMA32(wf[buf][1], hf[buf][1], acc[1][1]);
    issue1(SN{}, std::integral_constant<int, 3>{}); SB();
  });
  STAMPW(1, 6);
  const float s0 = 1.f - g0, s1 = 1.f - g1;
#pragma unroll
  for (int ct = 0; ct < 2; ++ct) { acc[ct][0] *= s0; acc[ct][1] *= s1; }
  const int i = rt >> 1;
  {
    GAS unsigned* fl = (GAS unsigned*)(ws + WS_FLAG) + (i * 16 + 4 * w + (lane & 3)) * 64;
    for (unsigned spins = 0; spins < (1u << 22); ++spins) {
      const unsigned v = __hip_atomic_load(fl, __ATOMIC_RELAXED, __HIP_MEMORY_SCOPE_AGENT);
      if (__all(v == 1u)) break;
      __builtin_amdgcn_s_sleep(1);
    }
    asm volatile("" ::: "memory");
  }
  STAMPW(1, 7);
  u32x4 of[8][2];
  {
    const auto rs = __builtin_amdgcn_make_buffer_rsrc((void*)(ws + WS_O), 0, 64 * 16 * 2048, 0x00020000);
#pragma unroll
    for (int o = 0; o < 8; ++o)
#pragma unroll
      for (int tt = 0; tt < 2; ++tt)
        of[o][tt] = __builtin_amdgcn_raw_buffer_load_b128(rs, ((tb0 + tt) * 16 + 4 * w + (o >> 1)) * 2048 + (o & 1) * 1024 + lane * 16, 0, 16);
  }
  wait_vm<0>();
  STAMPW(1, 8);
  static_for<28, 36>([&](auto S) {
    constexpr int s = S, buf = s & 1, o = s - 28;
    ldw(S);
#pragma unroll
    for (int ct = 0; ct < 2; ++ct)
#pragma unroll
      for (int tt = 0; tt < 2; ++tt) acc[ct][tt] = MFMA32(wf[buf][ct], __builtin_bit_cast(bf16x8, of[o][tt]), acc[ct][tt]);
  });
}

__global__ __launch_bounds__(512, 2) void k2_attn_out(const bf16* __restrict__ w2, const bf16* __restrict__ wo, char* __restrict__ ws, bf16* __restrict__ out) {
  __shared__ __attribute__((aligned(16))) char smem[K2_LDS];
  LAS char* lds = (LAS char*)smem;
  const int b = blockIdx.x, xcd = b & 7, idx = b >> 3, r = xcd >> 1, c2 = xcd & 1;
  const int tid = threadIdx.x, lane = tid & 63;
  const int w = __builtin_amdgcn_readfirstlane(tid >> 6);
  const bool has_gemm = idx < 28;
  const int rt = 8 * r + 4 * c2 + idx / 7, ctile = idx % 7;
  if (tid == 256) *(LAS unsigned*)(lds + K2_CNT) = 0u;
  if (w >= 4) {
    k2_attention(lds, ws, 4 * r + (idx >> 3), 8 * c2 + (idx & 7), w - 4, lane);
  } else if (has_gemm) {
    f32x16 acc[2][2] = {};
    k2_gemm(lds, (const char*)w2, (const char*)wo, ws, rt, ctile, w, lane, acc);
    wait_lgkm0();
    LAS char* red = lds + w * K2_RING;
    const int hh = lane >> 5;
#pragma unroll
    for (int ct = 0; ct < 2; ++ct)
#pragma unroll
      for (int tt = 0; tt < 2; ++tt)
#pragma unroll
        for (int rg = 0; rg < 4; ++rg) {
          const f32x4 v = {acc[ct][tt][4 * rg], acc[ct][tt][4 * rg + 1], acc[ct][tt][4 * rg + 2], acc[ct][tt][4 * rg + 3]};
          *(LAS f32x4*)(red + (32 * tt + (lane & 31)) * RED_STRIDE + (32 * ct + 8 * rg + 4 * hh) * 4) = v;
        }
  } else {
    raw_barrier();
  }
  STAMPW(1, 9);
  if (!has_gemm) return;
  __syncthreads();
  STAMPW(1, 10);
  {
    const int tok = tid >> 3, cg = tid & 7;
    f32x4 a = {0, 0, 0, 0}, c = {0, 0, 0, 0};
#pragma unroll
    for (int ww = 0; ww < 4; ++ww) {
      a += *(LAS const f32x4*)(lds + ww * K2_RING + tok * RED_STRIDE + cg * 32);
      c += *(LAS const f32x4*)(lds + ww * K2_RING + tok * RED_STRIDE + cg * 32 + 16);
    }
    const u32x4 v = {pk2(a[0], a[1]), pk2(a[2], a[3]), pk2(c[0], c[1]), pk2(c[2], c[3])};
    *(u32x4*)((char*)out + (size_t)(rt * 64 + tok) * 896 + ctile * 128 + cg * 16) = v;
  }
}

extern "C" void kernel_launch(void* const* d_in, const int* in_sizes, int n_in, void* d_out, int out_size, void* d_ws, size_t ws_size, hipStream_t stream) {
  const bf16* x = (const bf16*)d_in[0]; const bf16* wqkv = (const bf16*)d_in[1]; const bf16* wo = (const bf16*)d_in[2];
  const bf16* w1 = (const bf16*)d_in[4]; const bf16* w2 = (const bf16*)d_in[5]; const bf16* gw = (const bf16*)d_in[6];
  k1_proj<<<256, 512, 0, stream>>>(x, wqkv, w1, gw, (char*)d_ws);
  k2_attn_out<<<256, 512, 0, stream>>>(w2, wo, (char*)d_ws, (bf16*)d_out);
}
```

```cpp
#include <hip/hip_runtime.h>
#include <hip/hip_bf16.h>
#include <utility>
#include <type_traits>

typedef __hip_bfloat16 bf16;
typedef short bf16x8 __attribute__((ext_vector_type(8)));
typedef short s16x4 __attribute__((ext_vector_type(4)));
typedef float f32x4 __attribute__((ext_vector_type(4)));
typedef float f32x2 __attribute__((ext_vector_type(2)));
typedef float f32x16 __attribute__((ext_vector_type(16)));
typedef unsigned u32x4 __attribute__((ext_vector_type(4)));
typedef unsigned u32x2 __attribute__((ext_vector_type(2)));
typedef __bf16 bf16x2_t __attribute__((ext_vector_type(2)));
#define LAS __attribute__((address_space(3)))
#define GAS __attribute__((address_space(1)))
#define DI __device__ __forceinline__

constexpr int S_ = 2048, D_ = 448, NQKV = 1344, DFF = 1792;
constexpr size_t WS_FLAG = 0;
constexpr size_t WS_G    = 65536;
constexpr size_t WS_Q    = 131072;
constexpr size_t FRAG_TB = 28 * 1024;
constexpr size_t WS_K    = WS_Q + 64 * FRAG_TB;
constexpr size_t WS_V    = WS_K + 64 * FRAG_TB;
constexpr size_t WS_H    = WS_V + 64 * FRAG_TB + 4096;
constexpr size_t WS_O    = WS_H + 64 * 112 * 1024;

#define STAMPW(k, j) do {} while (0)
template <int I, int N, class F> DI void static_for(F&& f) {
  if constexpr (I < N) { f(std::integral_constant<int, I>{}); static_for<I + 1, N>(f); }
}
template <int N> DI void wait_vm() { asm volatile("s_waitcnt vmcnt(%0)" ::"n"(N) : "memory"); }
DI void wait_lgkm0() { asm volatile("s_waitcnt lgkmcnt(0)" ::: "memory"); }
DI void raw_barrier() { asm volatile("s_barrier" ::: "memory"); }
DI void glds16(const void* sbase, unsigned voff, unsigned lds_addr) {
  unsigned keep;
  asm volatile("s_mov_b32 %0, m0\n\ts_mov_b32 m0, %3\n\ts_nop 0\n\tglobal_load_lds_dwordx4 %1, %2\n\ts_mov_b32 m0, %0"
               : "=&s"(keep) : "v"(voff), "s"(sbase), "s"(lds_addr) : "memory");
}
DI unsigned pk2(float a, float b) {
  bf16x2_t r = __builtin_convertvector((f32x2){a, b}, bf16x2_t);
  return __builtin_bit_cast(unsigned, r);
}
DI float bf2f(unsigned short u) { return __uint_as_float(((unsigned)u) << 16); }
typedef short v4i16_t __attribute__((ext_vector_type(4)));
DI s16x4 ldtr(LAS const char* p) { return __builtin_bit_cast(s16x4, __builtin_amdgcn_ds_read_tr16_b64_v4i16((LAS v4i16_t*)p)); }
DI bf16x8 cat(s16x4 lo, s16x4 hi) { return __builtin_shufflevector(lo, hi, 0, 1, 2, 3, 4, 5, 6, 7); }
#define MFMA16(a, b, c) __builtin_amdgcn_mfma_f32_16x16x32_bf16(a, b, c, 0, 0, 0)
#define MFMA32(a, b, c) __builtin_amdgcn_mfma_f32_32x32x16_bf16(a, b, c, 0, 0, 0)

constexpr int K1_XB = 16384, K1_WB = 28672, K1_SLOT = K1_XB + K1_WB;
constexpr int K1_LDS = 3 * K1_SLOT;

#define SB() __builtin_amdgcn_sched_barrier(0)
typedef short s16x2 __attribute__((ext_vector_type(2)));
DI unsigned relu2(unsigned x) { s16x2 v = __builtin_bit_cast(s16x2, x); v = __builtin_elementwise_max(v, (s16x2){0, 0}); return __builtin_bit_cast(unsigned, v); }

DI void gld16(u32x4& d, const void* sbase, unsigned voff) {
  asm volatile("global_load_dwordx4 %0, %1, %2" : "=v"(d) : "v"(voff), "s"(sbase) : "memory");
}
DI void k1_loader(LAS char* lds, const char* xg, const char* wg, int ldw2, int w, char* ws) {
  const int lane = threadIdx.x & 63;
  const unsigned lds0 = (unsigned)(size_t)lds;
  unsigned xo[4], wo_[7];
#pragma unroll
  for (int tt = 0; tt < 4; ++tt) xo[tt] = (32 * w + 8 * tt + (lane >> 3)) * 896 + 16 * ((lane & 7) ^ ((lane >> 4) | (4 * (tt & 1))));
#pragma unroll
  for (int tt = 0; tt < 7; ++tt) { const int q = (7 * w + tt) * 64 + lane, rho = q / 28, cp = q % 28; wo_[tt] = rho * ldw2 + 16 * (cp ^ (2 * ((rho >> 3) & 1))); }
  auto stage = [&](int kc, int slot) {
    const char* xs = xg + kc * 128; const char* wsrc = wg + (size_t)kc * 64 * ldw2;
    const unsigned lb = lds0 + slot * K1_SLOT;
#pragma unroll
    for (int tt = 0; tt < 4; ++tt) glds16(xs, xo[tt], lb + (4 * w + tt) * 1024);
#pragma unroll
    for (int tt = 0; tt < 7; ++tt) glds16(wsrc, wo_[tt], lb + K1_XB + (7 * w + tt) * 1024);
  };
  STAMPW(0, 0);
  stage(0, 0); stage(1, 1);
  wait_vm<11>(); raw_barrier();
  STAMPW(0, 1);
  stage(2, 2);
  static_for<0, 6>([&](auto C) {
    constexpr int c = C;
    if constexpr (c + 2 <= 6) wait_vm<11>(); else wait_vm<0>();
    raw_barrier();
    if constexpr (c + 3 <= 6) stage(c + 3, c % 3);
  });
  STAMPW(0, 2);
}

template <bool VT>
DI void k1_compute(LAS char* lds, int kind  , int i, int cgl, int w, char* ws, const char* xg, const char* wg, int ldw2, int cc) {
  const int lane = threadIdx.x & 63;
  const int wm = w >> 1, wn = w & 1;
  const int m = lane & 15, g = lane >> 4, q4 = (lane & 15) >> 2, p4 = lane & 3;
  const unsigned xb0 = (64 * wm + m) * 128 + 16 * ((g) ^ (m >> 1));
  const unsigned xb1 = (64 * wm + m) * 128 + 16 * ((4 + g) ^ (m >> 1));
  const unsigned wbb = K1_XB + (8 * g + q4) * 448 + 8 * p4 + 7 * wn * 32;
  const unsigned wE = wbb + ((wn & 1) ? -32 * (g & 1) : 32 * (g & 1));
  const unsigned wO = wbb + ((wn & 1) ? 32 * (g & 1) : -32 * (g & 1));
  f32x4 acc[4][7];
#pragma unroll
  for (int a = 0; a < 4; ++a)
#pragma unroll
    for (int t = 0; t < 7; ++t) acc[a][t] = (f32x4){0.f, 0.f, 0.f, 0.f};
  bf16x8 xf[2][4]; s16x4 wl[2][7], wh[2][7];
  auto rd = [&](auto BUF, auto SLOT, auto KK, auto R) {
    constexpr int buf = BUF, slot = SLOT, kk = KK, r = R;
    LAS const char* base = lds + slot * K1_SLOT;
    if constexpr (r < 4) xf[buf][r] = *(LAS const bf16x8*)(base + (kk ? xb1 : xb0) + r * 2048);
    else {
      constexpr int tl = (r - 4) >> 1, hi = (r - 4) & 1;
      const unsigned b = ((tl & 1) ? wO : wE) + tl * 32 + kk * 32 * 448 + hi * 4 * 448;
      if constexpr (hi) wh[buf][tl] = ldtr(base + b); else wl[buf][tl] = ldtr(base + b);
    }
  };
  auto mm = [&](auto BUF, auto J) {
    constexpr int buf = BUF, j = J, tl = j >> 2, a = j & 3;
    const bf16x8 wf = cat(wl[buf][tl], wh[buf][tl]);
    acc[a][tl] = VT ? MFMA16(xf[buf][a], wf, acc[a][tl]) : MFMA16(wf, xf[buf][a], acc[a][tl]);
  };
  using I0 = std::integral_constant<int, 0>; using I1 = std::integral_constant<int, 1>;
  unsigned t0, t1, t2;
  {
    const int il = i & 3, e = w * 64 + lane;
    const char* wl = wg - ((size_t)wg & 64);
    const unsigned o0 = (4 * (e >> 2) + il) * ldw2 + (e & 3) * 128;
    const unsigned o1 = (4 * (64 + (e >> 2) % 48) + il) * ldw2 + (e & 3) * 128;
    const unsigned o2 = (e & 127) * 896 + cc * 128;
    asm volatile("global_load_dword %0, %3, %6\n\tglobal_load_dword %1, %4, %6\n\tglobal_load_dword %2, %5, %7"
                 : "=&v"(t0), "=&v"(t1), "=&v"(t2) : "v"(o0), "v"(o1), "v"(o2), "s"(wl), "s"(xg) : "memory");
  }
  STAMPW(0, 0);
  raw_barrier();
  STAMPW(0, 1);
  static_for<0, 18>([&](auto R) { rd(I0{}, I0{}, I0{}, R); });
  SB();
  static_for<0, 7>([&](auto C) {
    constexpr int c = C, slot = c % 3, nslot = (c + 1) % 3;
    static_for<0, 28>([&](auto J) {
      mm(I0{}, J);
      if constexpr (J < 18) rd(I1{}, std::integral_constant<int, slot>{}, I1{}, J);
      SB();
    });
    if constexpr (c < 6) {
      wait_lgkm0(); raw_barrier(); SB();
      static_for<0, 28>([&](auto J) {
        mm(I1{}, J);
        if constexpr (J < 18) rd(I0{}, std::integral_constant<int, nslot>{}, I0{}, J);
        SB();
      });
    } else {
      static_for<0, 28>([&](auto J) { mm(I1{}, J); });
    }
  });
  STAMPW(0, 8);
  wait_vm<0>(); asm volatile("" :: "v"(t0), "v"(t1), "v"(t2));
  const int tb0 = 4 * i + 2 * wm;
  if constexpr (!VT) {
    char* dst; int ntile;
    if (kind == 0) { dst = ws + WS_Q; ntile = 28; }
    else if (kind == 1) { dst = ws + WS_K; ntile = 28; }
    else { dst = ws + WS_H; ntile = 112; }
    const int T0 = cgl * 14 + 7 * wn;
    dst += ((size_t)tb0 * ntile + T0) * 1024 + lane * 16;
    if (kind == 0) {
      const float sc = 0.18898223650461362f * 1.4426950408889634f;
#pragma unroll
      for (int tl = 0; tl < 7; ++tl)
#pragma unroll
        for (int a = 0; a < 4; ++a) acc[a][tl] *= sc;
    }
    const bool relu = kind == 3;
#pragma unroll
    for (int tl = 0; tl < 7; ++tl)
#pragma unroll
      for (int pr = 0; pr < 2; ++pr) {
        const f32x4 X = acc[2 * pr][tl], Y = acc[2 * pr + 1][tl];
        unsigned x0 = pk2(X[0], X[1]), x1 = pk2(X[2], X[3]), y0 = pk2(Y[0], Y[1]), y1 = pk2(Y[2], Y[3]);
        if (relu) { x0 = relu2(x0); x1 = relu2(x1); y0 = relu2(y0); y1 = relu2(y1); }
        auto r0 = __builtin_amdgcn_permlane16_swap(x0, y0, false, false);
        auto r1 = __builtin_amdgcn_permlane16_swap(x1, y1, false, false);
        u32x4 v = {r0[0], r1[0], r0[1], r1[1]};
        *(u32x4*)(dst + ((size_t)pr * ntile + tl) * 1024) = v;
      }
  } else {
    const int row = lane >> 4;
    char* dst = ws + WS_V + ((size_t)((2 * tb0 + (row & 1)) * 2 + (row >> 1)) * 448 + cgl * 224 + 112 * wn + (lane & 15)) * 16;
#pragma unroll
    for (int tl = 0; tl < 7; ++tl)
#pragma unroll
      for (int pr = 0; pr < 2; ++pr) {
        const f32x4 X = acc[2 * pr][tl], Y = acc[2 * pr + 1][tl];
        unsigned x0 = pk2(X[0], X[1]), x1 = pk2(X[2], X[3]), y0 = pk2(Y[0], Y[1]), y1 = pk2(Y[2], Y[3]);
        auto r0 = __builtin_amdgcn_permlane16_swap(x0, y0, false, false);
        auto r1 = __builtin_amdgcn_permlane16_swap(x1, y1, false, false);
        u32x4 v = {r0[0], r1[0], r0[1], r1[1]};
        *(u32x4*)(dst + (size_t)(pr * 4 * 448 + 16 * tl) * 16) = v;
      }
  }
  STAMPW(0, 9);
}

__global__ __launch_bounds__(512, 2) void k1_proj(const bf16* __restrict__ x, const bf16* __restrict__ wqkv, const bf16* __restrict__ w1,
                                                  const bf16* __restrict__ gw, const bf16* __restrict__ w2, const bf16* __restrict__ wo, char* __restrict__ ws) {
  __shared__ __attribute__((aligned(16))) char smem[K1_LDS];
  const int b = blockIdx.x, xcd = b & 7, idx = b >> 3, r = xcd >> 1, c2 = xcd & 1;
  const int w = __builtin_amdgcn_readfirstlane(threadIdx.x >> 6);
  if (idx < 28) {
    const int i = 4 * r + idx / 7, cc = idx % 7;
    LAS char* lds = (LAS char*)smem;
    if (w >= 4) {
      const char* xg = (const char*)x + (size_t)i * 128 * 896;
      if (cc < 3) k1_loader(lds, xg, (const char*)wqkv + (2 * cc + c2) * 448, NQKV * 2, w - 4, ws);
      else k1_loader(lds, xg, (const char*)w1 + (4 * c2 + cc - 3) * 448, DFF * 2, w - 4, ws);
    } else {
      const char* xg = (const char*)x + (size_t)i * 128 * 896;
      const char* wg = cc < 3 ? (const char*)wqkv + (2 * cc + c2) * 448 : (const char*)w1 + (4 * c2 + cc - 3) * 448;
      const int ldw2 = cc < 3 ? NQKV * 2 : DFF * 2;
      if (cc == 2) k1_compute<true>(lds, 2, i, c2, w, ws, xg, wg, ldw2, cc);
      else k1_compute<false>(lds, cc < 3 ? cc : 3, i, cc < 3 ? c2 : 4 * c2 + cc - 3, w, ws, xg, wg, ldw2, cc);
    }
  } else {
    const int gi = (idx - 28) + 4 * xcd, tid = threadIdx.x, lane = tid & 63;
    if (tid < 8) ((unsigned*)(ws + WS_FLAG))[(gi * 8 + tid) * 64] = 0u;
    if (w < 4) {
      const int row = gi * 64 + w * 16 + (lane >> 2), part = lane & 3;
      float s = 0.f;
#pragma unroll
      for (int k = 0; k < 14; ++k) {
        const u32x4 xv = *(const u32x4*)((const char*)x + (size_t)row * 896 + k * 64 + part * 16);
        const u32x4 gv = *(const u32x4*)((const char*)gw + k * 64 + part * 16);
#pragma unroll
        for (int e = 0; e < 4; ++e) {
          s += __uint_as_float(xv[e] << 16) * __uint_as_float(gv[e] << 16);
          s += __uint_as_float(xv[e] & 0xffff0000u) * __uint_as_float(gv[e] & 0xffff0000u);
        }
      }
      s += __shfl_xor(s, 1); s += __shfl_xor(s, 2);
      if (part == 0) ((float*)(ws + WS_G))[row] = 1.f / (1.f + __expf(-s));
    }
    {
      const int part4 = idx - 28;
      unsigned acc = 0;
#pragma unroll
      for (int k = 0; k < 5; ++k) {
        const int e = k * 512 + tid, rr = part4 * 560 + (e >> 2), ln = e & 3;
        if ((e >> 2) < 560 && (c2 == 0 || ln < 3)) {
          const char* src = rr < 1792 ? (const char*)w2 + (size_t)rr * 896 : (const char*)wo + (size_t)(rr - 1792) * 896;
          acc ^= *(const unsigned*)(src + c2 * 512 + ln * 128);
        }
      }
      if (acc == 0x9e3779b9u) ((unsigned*)(ws + WS_G))[4096 + tid] = acc;
    }
  }
}

constexpr int K2_D = 11, K2_SLOT = 2048, K2_RING = 24576;
constexpr int K2_K = 4 * K2_RING, K2_V = K2_K + 12 * 2048, K2_CNT = K2_V + 12 * 2048, K2_LDS = K2_CNT + 16;
constexpr int RED_STRIDE = 272;

DI void k2_attention(LAS char* lds, char* ws, int i, int h, int w4, int lane) {
  const unsigned lds0 = (unsigned)(size_t)lds;
  STAMPW(1, 0);
  const int u0 = (h & 1) ? (28 * h - 4) / 8 : (28 * h) / 8;
  const int hh = lane >> 5, ql = lane & 31;
  {
    const unsigned kvo = lane * 16, vvo = (hh * 448 + 28 * h + ql) * 16;
#pragma unroll
    for (int bq = 0; bq < 3; ++bq) {
      const int bb = 3 * w4 + bq; int tb = 4 * i - 8 + bb; tb = tb < 0 ? 0 : tb;
#pragma unroll
      for (int s = 0; s < 2; ++s) {
        glds16(ws + WS_K + (size_t)tb * FRAG_TB + (u0 + 2 * s) * 512, kvo, lds0 + K2_K + bb * 2048 + s * 1024);
        glds16(ws + WS_V + (size_t)(2 * tb + s) * 2 * 448 * 16, vvo, lds0 + K2_V + bb * 2048 + s * 1024);
      }
    }
  }
  const int tbq = 4 * i + w4;
  u32x4 q0 = *(const u32x4*)(ws + WS_Q + (size_t)tbq * FRAG_TB + u0 * 512 + lane * 16);
  u32x4 q1 = *(const u32x4*)(ws + WS_Q + (size_t)tbq * FRAG_TB + (u0 + 2) * 512 + lane * 16);
  const float gq = ((const float*)(ws + WS_G))[tbq * 32 + ql];
  if (h & 1) { if (hh == 0) { q0[0] = 0; q0[1] = 0; } } else { if (hh == 1) { q1[2] = 0; q1[3] = 0; } }
  wait_vm<0>();
  if (ql >= 28) {
    const u32x4 ones = {0x3f803f80u, 0x3f803f80u, 0x3f803f80u, 0x3f803f80u};
#pragma unroll
    for (int bq = 0; bq < 3; ++bq)
#pragma unroll
      for (int s = 0; s < 2; ++s) *(LAS u32x4*)(lds + K2_V + (3 * w4 + bq) * 2048 + s * 1024 + lane * 16) = ones;
  }
  wait_lgkm0();
  STAMPW(1, 1);
  {
    if (lane == 0) __hip_atomic_fetch_add((LAS unsigned*)(lds + K2_CNT + 4), 1u, __ATOMIC_RELAXED, __HIP_MEMORY_SCOPE_WORKGROUP);
    while (__hip_atomic_load((LAS unsigned*)(lds + K2_CNT + 4), __ATOMIC_RELAXED, __HIP_MEMORY_SCOPE_WORKGROUP) < 4u) __builtin_amdgcn_s_sleep(1);
    asm volatile("" ::: "memory");
  }
  STAMPW(1, 2);
  const bf16x8 qa = __builtin_bit_cast(bf16x8, q0), qb = __builtin_bit_cast(bf16x8, q1);
  const int sw = (ql & 0x13) | ((ql & 4) << 1) | ((ql & 8) >> 1);
  LAS const char* kp = lds + K2_K + w4 * 2048 + hh * 512 + sw * 16;
  LAS const char* vp = lds + K2_V + w4 * 2048 + lane * 16;
  const f32x16 zero = {};
  const float NEG = -1e30f;
  auto mask0 = [&](f32x16& s) {
#pragma unroll
    for (int r = 0; r < 16; ++r) { const int kap = (r & 7) + 16 * (r >> 3) + 8 * hh; s[r] = (kap > ql) ? s[r] : NEG; } };
  auto mask8 = [&](f32x16& s) {
#pragma unroll
    for (int r = 0; r < 16; ++r) { const int kap = (r & 7) + 16 * (r >> 3) + 8 * hh; s[r] = (kap <= ql) ? s[r] : NEG; } };
  auto kfrag = [&](int j, int s) { return *(LAS const bf16x8*)(kp + j * 2048 + s * 1024); };
  auto vfrag = [&](int j, int s) { return *(LAS const bf16x8*)(vp + j * 2048 + s * 1024); };
  auto max16 = [&](const f32x16& s, float& m0, float& m1) {
#pragma unroll
    for (int r = 0; r < 16; r += 4) { m0 = fmaxf(fmaxf(m0, s[r]), s[r + 1]); m1 = fmaxf(fmaxf(m1, s[r + 2]), s[r + 3]); } };
  auto expack = [&](const f32x16& s, bf16x8& p0, bf16x8& p1) {
    unsigned pw[8];
#pragma unroll
    for (int r = 0; r < 16; r += 2) pw[r >> 1] = pk2(__builtin_amdgcn_exp2f(s[r]), __builtin_amdgcn_exp2f(s[r + 1]));
    p0 = __builtin_bit_cast(bf16x8, (u32x4){pw[0], pw[1], pw[2], pw[3]});
    p1 = __builtin_bit_cast(bf16x8, (u32x4){pw[4], pw[5], pw[6], pw[7]}); };
  f32x16 o = zero;
  if (tbq >= 8) {
    float m0 = NEG, m1 = NEG;
    bf16x8 kf[2][2]; f32x16 sv[2];
    kf[0][0] = kfrag(0, 0); kf[0][1] = kfrag(0, 1);
    static_for<0, 10>([&](auto J) {
      constexpr int j = J;
      if constexpr (j + 1 < 9) { kf[(j + 1) & 1][0] = kfrag(j + 1, 0); kf[(j + 1) & 1][1] = kfrag(j + 1, 1); }
      if constexpr (j < 9) { sv[j & 1] = MFMA32(kf[j & 1][0], qa, zero); sv[j & 1] = MFMA32(kf[j & 1][1], qb, sv[j & 1]); }
      if constexpr (j >= 1) {
        if constexpr (j - 1 == 0) mask0(sv[0]);
        if constexpr (j - 1 == 8) mask8(sv[0]);
        max16(sv[(j - 1) & 1], m0, m1);
      }
      SB();
    });
    float mx = fmaxf(m0, m1);
    { auto rr = __builtin_amdgcn_permlane32_swap(__float_as_uint(mx), __float_as_uint(mx), false, false);
      mx = fmaxf(__uint_as_float(rr[0]), __uint_as_float(rr[1])); }
    f32x16 negm;
#pragma unroll
    for (int r = 0; r < 16; ++r) negm[r] = -mx;
    STAMPW(1, 3);
    bf16x8 vf[2];
    kf[0][0] = kfrag(0, 0); kf[0][1] = kfrag(0, 1);
    kf[1][0] = kfrag(1, 0); kf[1][1] = kfrag(1, 1);
    sv[0] = MFMA32(kf[0][0], qa, negm); sv[0] = MFMA32(kf[0][1], qb, sv[0]);
    SB();
    static_for<0, 9>([&](auto J) {
      constexpr int j = J;
      vf[0] = vfrag(j, 0); vf[1] = vfrag(j, 1);
      if constexpr (j + 1 < 9) { sv[(j + 1) & 1] = MFMA32(kf[(j + 1) & 1][0], qa, negm); sv[(j + 1) & 1] = MFMA32(kf[(j + 1) & 1][1], qb, sv[(j + 1) & 1]); }
      if constexpr (j + 2 < 9) { kf[j & 1][0] = kfrag(j + 2, 0); kf[j & 1][1] = kfrag(j + 2, 1); }
      if constexpr (j == 0) mask0(sv[0]);
      if constexpr (j == 8) mask8(sv[0]);
      bf16x8 p0, p1;
      expack(sv[j & 1], p0, p1);
      o = MFMA32(vf[0], p0, o); o = MFMA32(vf[1], p1, o);
      SB();
    });
  } else {
    const int j0 = 8 - tbq;
    float m0 = NEG, m1 = NEG;
    for (int j = j0; j < 9; ++j) {
      f32x16 s = MFMA32(kfrag(j, 0), qa, zero); s = MFMA32(kfrag(j, 1), qb, s);
      if (j == 8) mask8(s);
      max16(s, m0, m1);
    }
    float mx = fmaxf(m0, m1);
    { auto rr = __builtin_amdgcn_permlane32_swap(__float_as_uint(mx), __float_as_uint(mx), false, false);
      mx = fmaxf(__uint_as_float(rr[0]), __uint_as_float(rr[1])); }
    f32x16 negm;
#pragma unroll
    for (int r = 0; r < 16; ++r) negm[r] = -mx;
    for (int j = j0; j < 9; ++j) {
      f32x16 s = MFMA32(kfrag(j, 0), qa, negm); s = MFMA32(kfrag(j, 1), qb, s);
      if (j == 8) mask8(s);
      bf16x8 p0, p1;
      expack(s, p0, p1);
      o = MFMA32(vfrag(j, 0), p0, o); o = MFMA32(vfrag(j, 1), p1, o);
    }
  }
  float l;
  { auto rr = __builtin_amdgcn_permlane32_swap(__float_as_uint(o[12]), __float_as_uint(o[12]), false, false);
    l = hh ? o[12] : __uint_as_float(rr[1]); }
  const float scl = gq / l;
  u32x4 f0, f1;
  f0[0] = pk2(o[0] * scl, o[1] * scl); f0[1] = pk2(o[2] * scl, o[3] * scl); f0[2] = pk2(o[4] * scl, o[5] * scl); f0[3] = pk2(o[6] * scl, o[7] * scl);
  f1[0] = pk2(o[8] * scl, o[9] * scl); f1[1] = pk2(o[10] * scl, o[11] * scl); f1[2] = pk2(o[12] * scl, o[13] * scl); f1[3] = pk2(o[14] * scl, o[15] * scl);
  if (hh) { f1[2] = 0; f1[3] = 0; }
  {
    const auto rs = __builtin_amdgcn_make_buffer_rsrc((void*)(ws + WS_O), 0, 64 * 16 * 2048, 0x00020000);
    const unsigned off = (tbq * 16 + h) * 2048 + lane * 16;
    __builtin_amdgcn_raw_buffer_store_b128(f0, rs, off, 0, 16);
    __builtin_amdgcn_raw_buffer_store_b128(f1, rs, off + 1024, 0, 16);
  }
  STAMPW(1, 4);
  wait_vm<0>();
  STAMPW(1, 5);
  unsigned old = 0;
  if (lane == 0) old = __hip_atomic_fetch_add((LAS unsigned*)(lds + K2_CNT), 1u, __ATOMIC_RELAXED, __HIP_MEMORY_SCOPE_WORKGROUP);
  old = __builtin_amdgcn_readfirstlane(old);
  if (old == 3 && lane == 0)
    __hip_atomic_store((GAS unsigned*)(ws + WS_FLAG) + (i * 16 + h) * 64, 1u, __ATOMIC_RELAXED, __HIP_MEMORY_SCOPE_AGENT);
}

DI void k2_gemm(LAS char* lds, const char* w2, const char* wo, char* ws, int rt, int ctile, int w, int lane, f32x16 (&acc)[2][2]) {
  STAMPW(1, 0);
  const unsigned ring0 = (unsigned)(size_t)lds + w * K2_RING;
  LAS char* ring = lds + w * K2_RING;
  const int tb0 = 2 * rt;
  const char* hsrc = ws + WS_H + ((size_t)tb0 * 112 + 28 * w) * 1024;
  const char* wsrc = w2 + (size_t)(448 * w) * 896 + ctile * 128;
  const char* osrc = wo + ctile * 128;
  const unsigned hv = lane * 16;
  const unsigned chs = 16 * ((lane & 7) ^ (4 * ((lane >> 4) & 1)));
  const unsigned wv = (lane >> 3) * 896 + chs;
  const int orow = ((lane >> 3) & 3) + 8 * (lane >> 5);
  u32x4 hr[K2_D][2];
  auto issue1 = [&](auto S, auto Dn) {
    constexpr int s = S, d = Dn, slot = s % K2_D;
    const unsigned lb = ring0 + slot * K2_SLOT;
    if constexpr (s < 28) {
      if constexpr (d == 0) glds16(wsrc + (size_t)(16 * s) * 896, wv, lb);
      if constexpr (d == 1) glds16(wsrc + (size_t)(16 * s + 8) * 896, wv, lb + 1024);
      if constexpr (d == 2) gld16(hr[slot][0], hsrc + s * 1024, hv);
      if constexpr (d == 3) gld16(hr[slot][1], hsrc + s * 1024 + 112 * 1024, hv);
    } else if constexpr (s < 36) {
      if constexpr (d < 2) {
        constexpr int o = s - 28;
        const int rb = 28 * (4 * w + (o >> 1)) + 16 * (o & 1);
        const int ra = min(rb + orow + 4 * d, 447);
        glds16(osrc, ra * 896 + chs, lb + d * 1024);
      }
    }
  };
  auto issue = [&](auto S) { static_for<0, 4>([&](auto Dn) { issue1(S, Dn); }); };
  const int hh = lane >> 5, blk = (lane >> 4) & 1, q4 = (lane & 15) >> 2, p4 = lane & 3;
  const unsigned tb_0 = (8 * hh + q4) * 128 + 16 * ((0 + 2 * blk + (p4 >> 1)) ^ (4 * ((q4 >> 1) & 1))) + 8 * (p4 & 1);
  const unsigned tb_1 = (8 * hh + q4) * 128 + 16 * ((4 + 2 * blk + (p4 >> 1)) ^ (4 * ((q4 >> 1) & 1))) + 8 * (p4 & 1);
  bf16x8 wf[2][2];
  auto ldw0 = [&](auto S) { constexpr int s = S; LAS const char* b = ring + (s % K2_D) * K2_SLOT; wf[s & 1][0] = cat(ldtr(b + tb_0), ldtr(b + tb_0 + 512)); };
  auto ldw1 = [&](auto S) { constexpr int s = S; LAS const char* b = ring + (s % K2_D) * K2_SLOT; wf[s & 1][1] = cat(ldtr(b + tb_1), ldtr(b + tb_1 + 512)); };
  float g0, g1;
  asm volatile("global_load_dword %0, %2, %3\n\tglobal_load_dword %1, %2, %3 offset:128" : "=&v"(g0), "=&v"(g1) : "v"((lane & 31) * 4), "s"(ws + WS_G + tb0 * 128) : "memory");
  static_for<0, K2_D>([&](auto S) { issue(S); });
  STAMPW(1, 1);
  constexpr auto nops = [](int t) constexpr { return t < 28 ? 4 : (t < 36 ? 2 : 0); };
  wait_vm<4 * (K2_D - 1)>();
  STAMPW(1, 2);
  ldw0(std::integral_constant<int, 0>{}); ldw1(std::integral_constant<int, 0>{});
  static_for<0, 28>([&](auto S) {
    constexpr int s = S, buf = s & 1, slot = s % K2_D;
    wait_lgkm0();
    constexpr int allow = [] { int n = 0; for (int t = s + 2; t <= s + K2_D - 1; ++t) n += (t < 28 ? 4 : (t < 36 ? 2 : 0)); return n; }();
    wait_vm<allow>();
    asm volatile("" : "+v"(hr[slot][0]), "+v"(hr[slot][1]));
    const bf16x8 h0 = __builtin_bit_cast(bf16x8, hr[slot][0]), h1 = __builtin_bit_cast(bf16x8, hr[slot][1]);
    SB();
    using SN = std::integral_constant<int, s + K2_D>;
    using S1 = std::integral_constant<int, s + 1>;
    acc[0][0] = MFMA32(wf[buf][0], h0, acc[0][0]);
    ldw0(S1{});
    issue1(SN{}, std::integral_constant<int, 0>{}); SB();
    acc[0][1] = MFMA32(wf[buf][0], h1, acc[0][1]);
    ldw1(S1{});
    issue1(SN{}, std::integral_constant<int, 1>{}); SB();
    acc[1][0] = MFMA32(wf[buf][1], h0, acc[1][0]); SB();
    acc[1][1] = MFMA32(wf[buf][1], h1, acc[1][1]); SB();
    issue1(SN{}, std::integral_constant<int, 2>{});
    issue1(SN{}, std::integral_constant<int, 3>{}); SB();
  });
  STAMPW(1, 6);
  asm volatile("" : "+v"(g0), "+v"(g1));
  const float s0 = 1.f - g0, s1 = 1.f - g1;
#pragma unroll
  for (int ct = 0; ct < 2; ++ct) { acc[ct][0] *= s0; acc[ct][1] *= s1; }
  const int i = rt >> 1;
  {
    GAS unsigned* fl = (GAS unsigned*)(ws + WS_FLAG) + (i * 16 + 4 * w + (lane & 3)) * 64;
    for (unsigned spins = 0; spins < (1u << 22); ++spins) {
      const unsigned v = __hip_atomic_load(fl, __ATOMIC_RELAXED, __HIP_MEMORY_SCOPE_AGENT);
      if (__all(v == 1u)) break;
      __builtin_amdgcn_s_sleep(1);
    }
    asm volatile("" ::: "memory");
  }
  STAMPW(1, 7);
  u32x4 of[8][2];
  {
    const auto rs = __builtin_amdgcn_make_buffer_rsrc((void*)(ws + WS_O), 0, 64 * 16 * 2048, 0x00020000);
#pragma unroll
    for (int o = 0; o < 8; ++o)
#pragma unroll
      for (int tt = 0; tt < 2; ++tt)
        of[o][tt] = __builtin_amdgcn_raw_buffer_load_b128(rs, ((tb0 + tt) * 16 + 4 * w + (o >> 1)) * 2048 + (o & 1) * 1024 + lane * 16, 0, 16);
  }
  STAMPW(1, 8);
  static_for<28, 36>([&](auto S) {
    constexpr int s = S, buf = s & 1, o = s - 28;
    if constexpr (s > 28) { ldw0(S); ldw1(S); }
#pragma unroll
    for (int ct = 0; ct < 2; ++ct)
#pragma unroll
      for (int tt = 0; tt < 2; ++tt) acc[ct][tt] = MFMA32(wf[buf][ct], __builtin_bit_cast(bf16x8, of[o][tt]), acc[ct][tt]);
  });
}

__global__ __launch_bounds__(512, 2) void k2_attn_out(const bf16* __restrict__ w2, const bf16* __restrict__ wo, char* __restrict__ ws, bf16* __restrict__ out) {
  __shared__ __attribute__((aligned(16))) char smem[K2_LDS];
  LAS char* lds = (LAS char*)smem;
  const int b = blockIdx.x, xcd = b & 7, idx = b >> 3, r = xcd >> 1, c2 = xcd & 1;
  const int tid = threadIdx.x, lane = tid & 63;
  const int w = __builtin_amdgcn_readfirstlane(tid >> 6);
  const bool has_gemm = c2 == 0 || idx < 24;
  const int rt = 8 * r + (c2 ? idx / 3 : idx >> 2), ctile = c2 ? 4 + idx % 3 : (idx & 3);
  if (tid == 256) { *(LAS unsigned*)(lds + K2_CNT) = 0u; *(LAS unsigned*)(lds + K2_CNT + 4) = 0u; }
  __syncthreads();
  if (w >= 4) {
    __builtin_amdgcn_s_setprio(3);
    k2_attention(lds, ws, 4 * r + (idx >> 3), 8 * c2 + (idx & 7), w - 4, lane);
  } else if (has_gemm) {
    f32x16 acc[2][2] = {};
    k2_gemm(lds, (const char*)w2, (const char*)wo, ws, rt, ctile, w, lane, acc);
    wait_lgkm0();
    LAS char* red = lds + w * K2_RING;
    const int hh = lane >> 5;
#pragma unroll
    for (int ct = 0; ct < 2; ++ct)
#pragma unroll
      for (int tt = 0; tt < 2; ++tt)
#pragma unroll
        for (int rg = 0; rg < 4; ++rg) {
          const f32x4 v = {acc[ct][tt][4 * rg], acc[ct][tt][4 * rg + 1], acc[ct][tt][4 * rg + 2], acc[ct][tt][4 * rg + 3]};
          *(LAS f32x4*)(red + (32 * tt + (lane & 31)) * RED_STRIDE + (32 * ct + 8 * rg + 4 * hh) * 4) = v;
        }
  }
  STAMPW(1, 9);
  if (!has_gemm) return;
  __syncthreads();
  STAMPW(1, 10);
  {
    const int tok = tid >> 3, cg = tid & 7;
    f32x4 a = {0, 0, 0, 0}, c = {0, 0, 0, 0};
#pragma unroll
    for (int ww = 0; ww < 4; ++ww) {
      a += *(LAS const f32x4*)(lds + ww * K2_RING + tok * RED_STRIDE + cg * 32);
      c += *(LAS const f32x4*)(lds + ww * K2_RING + tok * RED_STRIDE + cg * 32 + 16);
    }
    const u32x4 v = {pk2(a[0], a[1]), pk2(a[2], a[3]), pk2(c[0], c[1]), pk2(c[2], c[3])};
    *(u32x4*)((char*)out + (size_t)(rt * 64 + tok) * 896 + ctile * 128 + cg * 16) = v;
  }
}

extern "C" void kernel_launch(void* const* d_in, const int* in_sizes, int n_in, void* d_out, int out_size, void* d_ws, size_t ws_size, hipStream_t stream) {
  const bf16* x = (const bf16*)d_in[0]; const bf16* wqkv = (const bf16*)d_in[1]; const bf16* wo = (const bf16*)d_in[2];
  const bf16* w1 = (const bf16*)d_in[4]; const bf16* w2 = (const bf16*)d_in[5]; const bf16* gw = (const bf16*)d_in[6];
  k1_proj<<<256, 512, 0, stream>>>(x, wqkv, w1, gw, w2, wo, (char*)d_ws);
  k2_attn_out<<<256, 512, 0, stream>>>(w2, wo, (char*)d_ws, (bf16*)d_out);
}
```

```cpp
#include <hip/hip_runtime.h>
#include <hip/hip_bf16.h>
#include <utility>
#include <type_traits>

typedef __hip_bfloat16 bf16;
typedef short bf16x8 __attribute__((ext_vector_type(8)));
typedef short s16x4 __attribute__((ext_vector_type(4)));
typedef float f32x4 __attribute__((ext_vector_type(4)));
typedef float f32x2 __attribute__((ext_vector_type(2)));
typedef float f32x16 __attribute__((ext_vector_type(16)));
typedef unsigned u32x4 __attribute__((ext_vector_type(4)));
typedef unsigned u32x2 __attribute__((ext_vector_type(2)));
typedef __bf16 bf16x2_t __attribute__((ext_vector_type(2)));
#define LAS __attribute__((address_space(3)))
#define GAS __attribute__((address_space(1)))
#define DI __device__ __forceinline__

constexpr int S_ = 2048, D_ = 448, NQKV = 1344, DFF = 1792;
constexpr size_t WS_FLAG = 0;
constexpr size_t WS_G    = 65536;
constexpr size_t WS_Q    = 131072;
constexpr size_t FRAG_TB = 28 * 1024;
constexpr size_t WS_K    = WS_Q + 64 * FRAG_TB;
constexpr size_t WS_V    = WS_K + 64 * FRAG_TB;
constexpr size_t WS_H    = WS_V + 64 * FRAG_TB + 4096;
constexpr size_t WS_O    = WS_H + 64 * 112 * 1024;

#define STAMP_DECL do {} while (0)
#define STAMPW(k, j) do {} while (0)
#define STAMP_FLUSH(k) do {} while (0)
#define STAMP_PARAM
#define STAMP_ARG
template <int I, int N, class F> DI void static_for(F&& f) {
  if constexpr (I < N) { f(std::integral_constant<int, I>{}); static_for<I + 1, N>(f); }
}
template <int N> DI void wait_vm() { asm volatile("s_waitcnt vmcnt(%0)" ::"n"(N) : "memory"); }
DI void wait_lgkm0() { asm volatile("s_waitcnt lgkmcnt(0)" ::: "memory"); }
DI void raw_barrier() { asm volatile("s_barrier" ::: "memory"); }
DI void glds16(const void* sbase, unsigned voff, unsigned lds_addr) {
  unsigned keep;
  asm volatile("s_mov_b32 %0, m0\n\ts_mov_b32 m0, %3\n\ts_nop 0\n\tglobal_load_lds_dwordx4 %1, %2\n\ts_mov_b32 m0, %0"
               : "=&s"(keep) : "v"(voff), "s"(sbase), "s"(lds_addr) : "memory");
}
DI unsigned pk2(float a, float b) {
  bf16x2_t r = __builtin_convertvector((f32x2){a, b}, bf16x2_t);
  return __builtin_bit_cast(unsigned, r);
}
DI float bf2f(unsigned short u) { return __uint_as_float(((unsigned)u) << 16); }
typedef short v4i16_t __attribute__((ext_vector_type(4)));
DI s16x4 ldtr(LAS const char* p) { return __builtin_bit_cast(s16x4, __builtin_amdgcn_ds_read_tr16_b64_v4i16((LAS v4i16_t*)p)); }
DI bf16x8 cat(s16x4 lo, s16x4 hi) { return __builtin_shufflevector(lo, hi, 0, 1, 2, 3, 4, 5, 6, 7); }
#define MFMA16(a, b, c) __builtin_amdgcn_mfma_f32_16x16x32_bf16(a, b, c, 0, 0, 0)
#define MFMA32(a, b, c) __builtin_amdgcn_mfma_f32_32x32x16_bf16(a, b, c, 0, 0, 0)

constexpr int K1_XB = 16384, K1_WB = 28672, K1_SLOT = K1_XB + K1_WB;
constexpr int K1_LDS = 3 * K1_SLOT;

#define SB() __builtin_amdgcn_sched_barrier(0)
typedef short s16x2 __attribute__((ext_vector_type(2)));
DI unsigned relu2(unsigned x) { s16x2 v = __builtin_bit_cast(s16x2, x); v = __builtin_elementwise_max(v, (s16x2){0, 0}); return __builtin_bit_cast(unsigned, v); }

DI void gld16(u32x4& d, const void* sbase, unsigned voff) {
  asm volatile("global_load_dwordx4 %0, %1, %2" : "=v"(d) : "v"(voff), "s"(sbase) : "memory");
}
DI void k1_loader(LAS char* lds, const char* xg, const char* wg, int ldw2, int w, char* ws STAMP_PARAM) {
  const int lane = threadIdx.x & 63;
  const unsigned lds0 = (unsigned)(size_t)lds;
  unsigned xo[4], wo_[7];
#pragma unroll
  for (int tt = 0; tt < 4; ++tt) xo[tt] = (32 * w + 8 * tt + (lane >> 3)) * 896 + 16 * ((lane & 7) ^ ((lane >> 4) | (4 * (tt & 1))));
#pragma unroll
  for (int tt = 0; tt < 7; ++tt) { const int q = (7 * w + tt) * 64 + lane, rho = q / 28, cp = q % 28; wo_[tt] = rho * ldw2 + 16 * (cp ^ (2 * ((rho >> 3) & 1))); }
  auto stage = [&](int kc, int slot) {
    const char* xs = xg + kc * 128; const char* wsrc = wg + (size_t)kc * 64 * ldw2;
    const unsigned lb = lds0 + slot * K1_SLOT;
#pragma unroll
    for (int tt = 0; tt < 4; ++tt) glds16(xs, xo[tt], lb + (4 * w + tt) * 1024);
#pragma unroll
    for (int tt = 0; tt < 7; ++tt) glds16(wsrc, wo_[tt], lb + K1_XB + (7 * w + tt) * 1024);
  };
  STAMPW(0, 0);
  stage(0, 0); stage(1, 1);
  wait_vm<11>(); raw_barrier();
  STAMPW(0, 1);
  stage(2, 2);
  static_for<0, 6>([&](auto C) {
    constexpr int c = C;
    if constexpr (c + 2 <= 6) wait_vm<11>(); else wait_vm<0>();
    raw_barrier();
    if constexpr (c + 3 <= 6) stage(c + 3, c % 3);
  });
  STAMPW(0, 2);
}

template <bool VT>
DI void k1_compute(LAS char* lds, int kind  , int i, int cgl, int w, char* ws, const char* xg, const char* wg, int ldw2, int cc STAMP_PARAM) {
  const int lane = threadIdx.x & 63;
  const int wm = w >> 1, wn = w & 1;
  const int m = lane & 15, g = lane >> 4, q4 = (lane & 15) >> 2, p4 = lane & 3;
  const unsigned xb0 = (64 * wm + m) * 128 + 16 * ((g) ^ (m >> 1));
  const unsigned xb1 = (64 * wm + m) * 128 + 16 * ((4 + g) ^ (m >> 1));
  const unsigned wbb = K1_XB + (8 * g + q4) * 448 + 8 * p4 + 7 * wn * 32;
  const unsigned wE = wbb + ((wn & 1) ? -32 * (g & 1) : 32 * (g & 1));
  const unsigned wO = wbb + ((wn & 1) ? 32 * (g & 1) : -32 * (g & 1));
  f32x4 acc[4][7];
#pragma unroll
  for (int a = 0; a < 4; ++a)
#pragma unroll
    for (int t = 0; t < 7; ++t) acc[a][t] = (f32x4){0.f, 0.f, 0.f, 0.f};
  bf16x8 xf[2][4]; s16x4 wl[2][7], wh[2][7];
  auto rd = [&](auto BUF, auto SLOT, auto KK, auto R) {
    constexpr int buf = BUF, slot = SLOT, kk = KK, r = R;
    LAS const char* base = lds + slot * K1_SLOT;
    if constexpr (r < 4) xf[buf][r] = *(LAS const bf16x8*)(base + (kk ? xb1 : xb0) + r * 2048);
    else {
      constexpr int tl = (r - 4) >> 1, hi = (r - 4) & 1;
      const unsigned b = ((tl & 1) ? wO : wE) + tl * 32 + kk * 32 * 448 + hi * 4 * 448;
      if constexpr (hi) wh[buf][tl] = ldtr(base + b); else wl[buf][tl] = ldtr(base + b);
    }
  };
  auto mm = [&](auto BUF, auto J) {
    constexpr int buf = BUF, j = J, tl = j >> 2, a = j & 3;
    const bf16x8 wf = cat(wl[buf][tl], wh[buf][tl]);
    acc[a][tl] = VT ? MFMA16(xf[buf][a], wf, acc[a][tl]) : MFMA16(wf, xf[buf][a], acc[a][tl]);
  };
  using I0 = std::integral_constant<int, 0>; using I1 = std::integral_constant<int, 1>;
  STAMPW(0, 0);
  raw_barrier();
  STAMPW(0, 1);
  unsigned t0, t1;
  {
    const int il = i & 3, e = w * 64 + lane;
    const char* wl = wg - ((size_t)wg & 64);
    const unsigned o0 = (192 + 4 * (e >> 2) + il) * ldw2 + (e & 3) * 128;
    const int xe = min(cc * 74 + (e % 74), 511);
    const unsigned o1 = (xe >> 2) * 896 + (3 + (xe & 3)) * 128;
    asm volatile("global_load_dword %0, %1, %2" : "=&v"(t0) : "v"(o0), "s"(wl) : "memory");
    t1 = 0;
    if (w < 2) asm volatile("global_load_dword %0, %1, %2" : "=&v"(t1) : "v"(o1), "s"(xg) : "memory");
  }
  static_for<0, 18>([&](auto R) { rd(I0{}, I0{}, I0{}, R); });
  SB();
  static_for<0, 7>([&](auto C) {
    constexpr int c = C, slot = c % 3, nslot = (c + 1) % 3;
    static_for<0, 28>([&](auto J) {
      mm(I0{}, J);
      if constexpr (J < 18) rd(I1{}, std::integral_constant<int, slot>{}, I1{}, J);
      SB();
    });
    if constexpr (c < 6) {
      wait_lgkm0(); raw_barrier(); SB();
      static_for<0, 28>([&](auto J) {
        mm(I1{}, J);
        if constexpr (J < 18) rd(I0{}, std::integral_constant<int, nslot>{}, I0{}, J);
        SB();
      });
    } else {
      static_for<0, 28>([&](auto J) { mm(I1{}, J); });
    }
  });
  STAMPW(0, 8);
  wait_vm<0>(); asm volatile("" :: "v"(t0), "v"(t1));
  const int tb0 = 4 * i + 2 * wm;
  if constexpr (!VT) {
    char* dst; int ntile;
    if (kind == 0) { dst = ws + WS_Q; ntile = 28; }
    else if (kind == 1) { dst = ws + WS_K; ntile = 28; }
    else { dst = ws + WS_H; ntile = 112; }
    const int T0 = cgl * 14 + 7 * wn;
    dst += ((size_t)tb0 * ntile + T0) * 1024 + lane * 16;
    if (kind == 0) {
      const float sc = 0.18898223650461362f * 1.4426950408889634f;
#pragma unroll
      for (int tl = 0; tl < 7; ++tl)
#pragma unroll
        for (int a = 0; a < 4; ++a) acc[a][tl] *= sc;
    }
    const bool relu = kind == 3;
#pragma unroll
    for (int tl = 0; tl < 7; ++tl)
#pragma unroll
      for (int pr = 0; pr < 2; ++pr) {
        const f32x4 X = acc[2 * pr][tl], Y = acc[2 * pr + 1][tl];
        unsigned x0 = pk2(X[0], X[1]), x1 = pk2(X[2], X[3]), y0 = pk2(Y[0], Y[1]), y1 = pk2(Y[2], Y[3]);
        if (relu) { x0 = relu2(x0); x1 = relu2(x1); y0 = relu2(y0); y1 = relu2(y1); }
        auto r0 = __builtin_amdgcn_permlane16_swap(x0, y0, false, false);
        auto r1 = __builtin_amdgcn_permlane16_swap(x1, y1, false, false);
        u32x4 v = {r0[0], r1[0], r0[1], r1[1]};
        *(u32x4*)(dst + ((size_t)pr * ntile + tl) * 1024) = v;
      }
  } else {
    const int row = lane >> 4;
    char* dst = ws + WS_V + ((size_t)((2 * tb0 + (row & 1)) * 2 + (row >> 1)) * 448 + cgl * 224 + 112 * wn + (lane & 15)) * 16;
#pragma unroll
    for (int tl = 0; tl < 7; ++tl)
#pragma unroll
      for (int pr = 0; pr < 2; ++pr) {
        const f32x4 X = acc[2 * pr][tl], Y = acc[2 * pr + 1][tl];
        unsigned x0 = pk2(X[0], X[1]), x1 = pk2(X[2], X[3]), y0 = pk2(Y[0], Y[1]), y1 = pk2(Y[2], Y[3]);
        auto r0 = __builtin_amdgcn_permlane16_swap(x0, y0, false, false);
        auto r1 = __builtin_amdgcn_permlane16_swap(x1, y1, false, false);
        u32x4 v = {r0[0], r1[0], r0[1], r1[1]};
        *(u32x4*)(dst + (size_t)(pr * 4 * 448 + 16 * tl) * 16) = v;
      }
  }
  STAMPW(0, 9);
}

__global__ __launch_bounds__(512, 2) void k1_proj(const bf16* __restrict__ x, const bf16* __restrict__ wqkv, const bf16* __restrict__ w1,
                                                  const bf16* __restrict__ gw, const bf16* __restrict__ w2, const bf16* __restrict__ wo, char* __restrict__ ws) {
  __shared__ __attribute__((aligned(16))) char smem[K1_LDS];
  const int b = blockIdx.x, xcd = b & 7, idx = b >> 3, r = xcd >> 1, c2 = xcd & 1;
  const int w = __builtin_amdgcn_readfirstlane(threadIdx.x >> 6);
  STAMP_DECL;
  if (idx < 28) {
    const int i = 4 * r + idx / 7, cc = idx % 7;
    LAS char* lds = (LAS char*)smem;
    if (w >= 4) {
      const char* xg = (const char*)x + (size_t)i * 128 * 896;
      if (cc < 3) k1_loader(lds, xg, (const char*)wqkv + (2 * cc + c2) * 448, NQKV * 2, w - 4, ws STAMP_ARG);
      else k1_loader(lds, xg, (const char*)w1 + (4 * c2 + cc - 3) * 448, DFF * 2, w - 4, ws STAMP_ARG);
    } else {
      const char* xg = (const char*)x + (size_t)i * 128 * 896;
      const char* wg = cc < 3 ? (const char*)wqkv + (2 * cc + c2) * 448 : (const char*)w1 + (4 * c2 + cc - 3) * 448;
      const int ldw2 = cc < 3 ? NQKV * 2 : DFF * 2;
      if (cc == 2) k1_compute<true>(lds, 2, i, c2, w, ws, xg, wg, ldw2, cc STAMP_ARG);
      else k1_compute<false>(lds, cc < 3 ? cc : 3, i, cc < 3 ? c2 : 4 * c2 + cc - 3, w, ws, xg, wg, ldw2, cc STAMP_ARG);
    }
    STAMP_FLUSH(0);
  } else {
    const int gi = (idx - 28) + 4 * xcd, tid = threadIdx.x, lane = tid & 63;
    if (tid < 8) ((unsigned*)(ws + WS_FLAG))[(gi * 8 + tid) * 64] = 0u;
    if (w < 4) {
      const int row = gi * 64 + w * 16 + (lane >> 2), part = lane & 3;
      float s = 0.f;
#pragma unroll
      for (int k = 0; k < 14; ++k) {
        const u32x4 xv = *(const u32x4*)((const char*)x + (size_t)row * 896 + k * 64 + part * 16);
        const u32x4 gv = *(const u32x4*)((const char*)gw + k * 64 + part * 16);
#pragma unroll
        for (int e = 0; e < 4; ++e) {
          s += __uint_as_float(xv[e] << 16) * __uint_as_float(gv[e] << 16);
          s += __uint_as_float(xv[e] & 0xffff0000u) * __uint_as_float(gv[e] & 0xffff0000u);
        }
      }
      s += __shfl_xor(s, 1); s += __shfl_xor(s, 2);
      if (part == 0) ((float*)(ws + WS_G))[row] = 1.f / (1.f + __expf(-s));
    }
    {
      const int part4 = idx - 28;
      unsigned acc = 0;
#pragma unroll
      for (int k = 0; k < 8; ++k) {
        const int e = k * 512 + tid, rr = part4 * 560 + e / 7, ln = e % 7;
        if (e < 560 * 7) {
          const char* src = rr < 1792 ? (const char*)w2 + (size_t)rr * 896 : (const char*)wo + (size_t)(rr - 1792) * 896;
          acc ^= *(const unsigned*)(src + ln * 128);
        }
      }
      if (acc == 0x9e3779b9u) ((unsigned*)(ws + WS_G))[4096 + tid] = acc;
    }
  }
}

#define KD 11
constexpr int K2_D = KD, K2_SLOT = 2048, K2_RING = 24576;
constexpr int K2_K = 4 * K2_RING, K2_V = K2_K + 12 * 2048, K2_CNT = K2_V + 12 * 2048, K2_LDS = K2_CNT + 16;
constexpr int RED_STRIDE = 272;

DI void k2_attention(LAS char* lds, char* ws, int i, int h, int w4, int lane, int ig   STAMP_PARAM) {
  const unsigned lds0 = (unsigned)(size_t)lds;
  STAMPW(1, 0);
  const int u0 = (h & 1) ? (28 * h - 4) / 8 : (28 * h) / 8;
  const int hh = lane >> 5, ql = lane & 31;
  {
    const unsigned kvo = lane * 16, vvo = (hh * 448 + 28 * h + ql) * 16;
#pragma unroll
    for (int bq = 0; bq < 3; ++bq) {
      const int bb = 3 * w4 + bq; int tb = 4 * i - 8 + bb; tb = tb < 0 ? 0 : tb;
#pragma unroll
      for (int s = 0; s < 2; ++s) {
        glds16(ws + WS_K + (size_t)tb * FRAG_TB + (u0 + 2 * s) * 512, kvo, lds0 + K2_K + bb * 2048 + s * 1024);
        glds16(ws + WS_V + (size_t)(2 * tb + s) * 2 * 448 * 16, vvo, lds0 + K2_V + bb * 2048 + s * 1024);
      }
    }
  }
  const int tbq = 4 * i + w4;
  u32x4 q0 = *(const u32x4*)(ws + WS_Q + (size_t)tbq * FRAG_TB + u0 * 512 + lane * 16);
  u32x4 q1 = *(const u32x4*)(ws + WS_Q + (size_t)tbq * FRAG_TB + (u0 + 2) * 512 + lane * 16);
  const float gq = ((const float*)(ws + WS_G))[tbq * 32 + ql];
  if (h & 1) { if (hh == 0) { q0[0] = 0; q0[1] = 0; } } else { if (hh == 1) { q1[2] = 0; q1[3] = 0; } }
  wait_vm<0>();
  if (ql >= 28) {
    const u32x4 ones = {0x3f803f80u, 0x3f803f80u, 0x3f803f80u, 0x3f803f80u};
#pragma unroll
    for (int bq = 0; bq < 3; ++bq)
#pragma unroll
      for (int s = 0; s < 2; ++s) *(LAS u32x4*)(lds + K2_V + (3 * w4 + bq) * 2048 + s * 1024 + lane * 16) = ones;
  }
  wait_lgkm0();
  STAMPW(1, 1);
  {
    if (lane == 0) __hip_atomic_fetch_add((LAS unsigned*)(lds + K2_CNT + 4), 1u, __ATOMIC_RELAXED, __HIP_MEMORY_SCOPE_WORKGROUP);
    for (unsigned sp = 0; sp < (1u << 20) && __hip_atomic_load((LAS unsigned*)(lds + K2_CNT + 4), __ATOMIC_RELAXED, __HIP_MEMORY_SCOPE_WORKGROUP) < 4u; ++sp) __builtin_amdgcn_s_sleep(1);
    asm volatile("" ::: "memory");
  }
  __builtin_amdgcn_s_setprio(0);
  STAMPW(1, 2);
  const bf16x8 qa = __builtin_bit_cast(bf16x8, q0), qb = __builtin_bit_cast(bf16x8, q1);
  const int sw = (ql & 0x13) | ((ql & 4) << 1) | ((ql & 8) >> 1);
  LAS const char* kp = lds + K2_K + w4 * 2048 + hh * 512 + sw * 16;
  LAS const char* vp = lds + K2_V + w4 * 2048 + lane * 16;
  const f32x16 zero = {};
  const float NEG = -1e30f;
  auto mask0 = [&](f32x16& s) {
#pragma unroll
    for (int r = 0; r < 16; ++r) { const int kap = (r & 7) + 16 * (r >> 3) + 8 * hh; s[r] = (kap > ql) ? s[r] : NEG; } };
  auto mask8 = [&](f32x16& s) {
#pragma unroll
    for (int r = 0; r < 16; ++r) { const int kap = (r & 7) + 16 * (r >> 3) + 8 * hh; s[r] = (kap <= ql) ? s[r] : NEG; } };
  auto kfrag = [&](int j, int s) { return *(LAS const bf16x8*)(kp + j * 2048 + s * 1024); };
  auto vfrag = [&](int j, int s) { return *(LAS const bf16x8*)(vp + j * 2048 + s * 1024); };
  auto max16 = [&](const f32x16& s, float& m0, float& m1) {
#pragma unroll
    for (int r = 0; r < 16; r += 4) { m0 = fmaxf(fmaxf(m0, s[r]), s[r + 1]); m1 = fmaxf(fmaxf(m1, s[r + 2]), s[r + 3]); } };
  auto expack = [&](const f32x16& s, bf16x8& p0, bf16x8& p1) {
    unsigned pw[8];
#pragma unroll
    for (int r = 0; r < 16; r += 2) pw[r >> 1] = pk2(__builtin_amdgcn_exp2f(s[r]), __builtin_amdgcn_exp2f(s[r + 1]));
    p0 = __builtin_bit_cast(bf16x8, (u32x4){pw[0], pw[1], pw[2], pw[3]});
    p1 = __builtin_bit_cast(bf16x8, (u32x4){pw[4], pw[5], pw[6], pw[7]}); };
  f32x16 o = zero;
  if (tbq >= 8) {
    float m0 = NEG, m1 = NEG;
    bf16x8 kf[3][2]; f32x16 sv[2];
    kf[0][0] = kfrag(0, 0); kf[0][1] = kfrag(0, 1); kf[1][0] = kfrag(1, 0); kf[1][1] = kfrag(1, 1);
    SB();
    static_for<0, 10>([&](auto J) {
      constexpr int j = J;
      if constexpr (j < 9) { sv[j & 1] = MFMA32(kf[j % 3][0], qa, zero); sv[j & 1] = MFMA32(kf[j % 3][1], qb, sv[j & 1]); }
      SB();
      if constexpr (j + 2 < 9) { kf[(j + 2) % 3][0] = kfrag(j + 2, 0); kf[(j + 2) % 3][1] = kfrag(j + 2, 1); }
      else if constexpr (j + 2 < 11) { kf[(j + 2) % 3][0] = kfrag(j + 2 - 9, 0); kf[(j + 2) % 3][1] = kfrag(j + 2 - 9, 1); }
      if constexpr (j >= 1) {
        if constexpr (j - 1 == 0) mask0(sv[0]);
        if constexpr (j - 1 == 8) mask8(sv[0]);
        max16(sv[(j - 1) & 1], m0, m1);
      }
      SB();
    });
    float mx = fmaxf(m0, m1);
    { auto rr = __builtin_amdgcn_permlane32_swap(__float_as_uint(mx), __float_as_uint(mx), false, false);
      mx = fmaxf(__uint_as_float(rr[0]), __uint_as_float(rr[1])); }
    f32x16 negm;
#pragma unroll
    for (int r = 0; r < 16; ++r) negm[r] = -mx;
    STAMPW(1, 3);
    bf16x8 vf[2][2];
    vf[0][0] = vfrag(0, 0); vf[0][1] = vfrag(0, 1);
    kf[2][0] = kfrag(2, 0); kf[2][1] = kfrag(2, 1);
    sv[0] = MFMA32(kf[0][0], qa, negm); sv[0] = MFMA32(kf[0][1], qb, sv[0]);
    SB();
    static_for<0, 9>([&](auto J) {
      constexpr int j = J;
      if constexpr (j + 1 < 9) { sv[(j + 1) & 1] = MFMA32(kf[(j + 1) % 3][0], qa, negm); sv[(j + 1) & 1] = MFMA32(kf[(j + 1) % 3][1], qb, sv[(j + 1) & 1]); }
      SB();
      if constexpr (j + 3 < 9) { kf[j % 3][0] = kfrag(j + 3, 0); kf[j % 3][1] = kfrag(j + 3, 1); }
      if constexpr (j + 1 < 9) { vf[(j + 1) & 1][0] = vfrag(j + 1, 0); vf[(j + 1) & 1][1] = vfrag(j + 1, 1); }
      if constexpr (j == 0) mask0(sv[0]);
      if constexpr (j == 8) mask8(sv[0]);
      f32x16& S = sv[j & 1];
      u32x4 pa, pb;
#pragma unroll
      for (int r = 0; r < 4; ++r) pa[r] = pk2(__builtin_amdgcn_exp2f(S[2 * r]), __builtin_amdgcn_exp2f(S[2 * r + 1]));
      SB();
      o = MFMA32(vf[j & 1][0], __builtin_bit_cast(bf16x8, pa), o);
      SB();
#pragma unroll
      for (int r = 0; r < 4; ++r) pb[r] = pk2(__builtin_amdgcn_exp2f(S[8 + 2 * r]), __builtin_amdgcn_exp2f(S[8 + 2 * r + 1]));
      SB();
      o = MFMA32(vf[j & 1][1], __builtin_bit_cast(bf16x8, pb), o);
      SB();
    });
  } else {
    const int j0 = 8 - tbq;
    float m0 = NEG, m1 = NEG;
    for (int j = j0; j < 9; ++j) {
      f32x16 s = MFMA32(kfrag(j, 0), qa, zero); s = MFMA32(kfrag(j, 1), qb, s);
      if (j == 8) mask8(s);
      max16(s, m0, m1);
    }
    float mx = fmaxf(m0, m1);
    { auto rr = __builtin_amdgcn_permlane32_swap(__float_as_uint(mx), __float_as_uint(mx), false, false);
      mx = fmaxf(__uint_as_float(rr[0]), __uint_as_float(rr[1])); }
    f32x16 negm;
#pragma unroll
    for (int r = 0; r < 16; ++r) negm[r] = -mx;
    for (int j = j0; j < 9; ++j) {
      f32x16 s = MFMA32(kfrag(j, 0), qa, negm); s = MFMA32(kfrag(j, 1), qb, s);
      if (j == 8) mask8(s);
      bf16x8 p0, p1;
      expack(s, p0, p1);
      o = MFMA32(vfrag(j, 0), p0, o); o = MFMA32(vfrag(j, 1), p1, o);
    }
  }
  STAMPW(1, 6);
  __builtin_amdgcn_s_setprio(3);
  float l;
  { auto rr = __builtin_amdgcn_permlane32_swap(__float_as_uint(o[12]), __float_as_uint(o[12]), false, false);
    l = hh ? o[12] : __uint_as_float(rr[1]); }
  const float scl = gq / l;
  u32x4 f0, f1;
  f0[0] = pk2(o[0] * scl, o[1] * scl); f0[1] = pk2(o[2] * scl, o[3] * scl); f0[2] = pk2(o[4] * scl, o[5] * scl); f0[3] = pk2(o[6] * scl, o[7] * scl);
  f1[0] = pk2(o[8] * scl, o[9] * scl); f1[1] = pk2(o[10] * scl, o[11] * scl); f1[2] = pk2(o[12] * scl, o[13] * scl); f1[3] = pk2(o[14] * scl, o[15] * scl);
  if (hh) { f1[2] = 0; f1[3] = 0; }
  {
    const auto rs = __builtin_amdgcn_make_buffer_rsrc((void*)(ws + WS_O), 0, 64 * 16 * 2048, 0x00020000);
    const unsigned off = (tbq * 16 + h) * 2048 + lane * 16;
    __builtin_amdgcn_raw_buffer_store_b128(f0, rs, off, 0, 16);
    __builtin_amdgcn_raw_buffer_store_b128(f1, rs, off + 1024, 0, 16);
  }
  STAMPW(1, 4);
  wait_vm<0>();
  STAMPW(1, 5);
  unsigned old = 0;
  if (lane == 0) old = __hip_atomic_fetch_add((LAS unsigned*)(lds + K2_CNT), 1u, __ATOMIC_RELAXED, __HIP_MEMORY_SCOPE_WORKGROUP);
  old = __builtin_amdgcn_readfirstlane(old);
  if (old == 3 && lane == 0)
    __hip_atomic_store((GAS unsigned*)(ws + WS_FLAG) + (i * 16 + h) * 64, 1u, __ATOMIC_RELAXED, __HIP_MEMORY_SCOPE_AGENT);
  if (w4 == 0 && ig >= 0) {
    GAS unsigned* fl = (GAS unsigned*)(ws + WS_FLAG) + (ig * 16 + (lane & 15)) * 64;
    for (unsigned spins = 0; spins < (1u << 18); ++spins) {
      const unsigned v = __hip_atomic_load(fl, __ATOMIC_RELAXED, __HIP_MEMORY_SCOPE_AGENT);
      if (__all(v == 1u)) break;
      __builtin_amdgcn_s_sleep(2);
    }
    asm volatile("" ::: "memory");
    if (lane == 0) __hip_atomic_store((LAS unsigned*)(lds + K2_CNT + 8), 1u, __ATOMIC_RELAXED, __HIP_MEMORY_SCOPE_WORKGROUP);
  }
}

#define GM(x) x
#define GL(x) x
DI void gld16_sc1(u32x4& d, const void* sbase, unsigned voff) {
  asm volatile("global_load_dwordx4 %0, %1, %2 sc1" : "=v"(d) : "v"(voff), "s"(sbase) : "memory");
}
DI void k2_gemm(LAS char* lds, const char* w2, const char* wo, char* ws, int rt, int ctile, int w, int lane, f32x16 (&acc)[2][2] STAMP_PARAM) {
  STAMPW(1, 0);
  const unsigned ring0 = (unsigned)(size_t)lds + w * K2_RING;
  LAS char* ring = lds + w * K2_RING;
  const int tb0 = 2 * rt;
  const char* hsrc = ws + WS_H + ((size_t)tb0 * 112 + 28 * w) * 1024;
  const char* ofr = ws + WS_O + ((size_t)tb0 * 16 + 4 * w) * 2048;
  const char* wsrc = w2 + (size_t)(448 * w) * 896 + ctile * 128;
  const char* osrc = wo + ctile * 128;
  const unsigned hv = lane * 16;
  const unsigned chs = 16 * ((lane & 7) ^ (4 * ((lane >> 4) & 1)));
  const unsigned wv = (lane >> 3) * 896 + chs;
  const int orow = ((lane >> 3) & 3) + 8 * (lane >> 5);
  u32x4 hr[K2_D][2];
  auto issue1 = [&](auto S, auto Dn) {
    constexpr int s = S, d = Dn, slot = s % K2_D;
    const unsigned lb = ring0 + slot * K2_SLOT;
    if constexpr (s < 28) {
      if constexpr (d == 0) glds16(wsrc + (size_t)(16 * s) * 896, wv, lb);
      if constexpr (d == 1) glds16(wsrc + (size_t)(16 * s + 8) * 896, wv, lb + 1024);
      if constexpr (d == 2) gld16(hr[slot][0], hsrc + s * 1024, hv);
      if constexpr (d == 3) gld16(hr[slot][1], hsrc + s * 1024 + 112 * 1024, hv);
    } else if constexpr (s < 36) {
      constexpr int o = s - 28;
      if constexpr (d < 2) {
        const int rb = 28 * (4 * w + (o >> 1)) + 16 * (o & 1);
        const int ra = min(rb + orow + 4 * d, 447);
        glds16(osrc, ra * 896 + chs, lb + d * 1024);
      }
      if constexpr (d == 2) gld16_sc1(hr[slot][0], ofr + o * 1024, hv);
      if constexpr (d == 3) gld16_sc1(hr[slot][1], ofr + o * 1024 + 16 * 2048, hv);
    }
  };
  auto issue = [&](auto S) { static_for<0, 4>([&](auto Dn) { issue1(S, Dn); }); };
  const int hh = lane >> 5, blk = (lane >> 4) & 1, q4 = (lane & 15) >> 2, p4 = lane & 3;
  const unsigned tb_0 = (8 * hh + q4) * 128 + 16 * ((0 + 2 * blk + (p4 >> 1)) ^ (4 * ((q4 >> 1) & 1))) + 8 * (p4 & 1);
  const unsigned tb_1 = (8 * hh + q4) * 128 + 16 * ((4 + 2 * blk + (p4 >> 1)) ^ (4 * ((q4 >> 1) & 1))) + 8 * (p4 & 1);
  bf16x8 wf[2][2];
  auto ldw0 = [&](auto S) { constexpr int s = S; LAS const char* b = ring + (s % K2_D) * K2_SLOT; wf[s & 1][0] = cat(ldtr(b + tb_0), ldtr(b + tb_0 + 512)); };
  auto ldw1 = [&](auto S) { constexpr int s = S; LAS const char* b = ring + (s % K2_D) * K2_SLOT; wf[s & 1][1] = cat(ldtr(b + tb_1), ldtr(b + tb_1 + 512)); };
  float g0, g1;
  asm volatile("global_load_dword %0, %2, %3\n\tglobal_load_dword %1, %2, %3 offset:128" : "=&v"(g0), "=&v"(g1) : "v"((lane & 31) * 4), "s"(ws + WS_G + tb0 * 128) : "memory");
  static_for<0, K2_D>([&](auto S) { issue(S); });
  STAMPW(1, 1);
  wait_vm<4 * (K2_D - 1)>();
  STAMPW(1, 2);
  ldw0(std::integral_constant<int, 0>{}); ldw1(std::integral_constant<int, 0>{});
  static_for<0, 36>([&](auto S) {
    constexpr int s = S, buf = s & 1, slot = s % K2_D;
    wait_lgkm0();
    constexpr int last = (s + K2_D - 1 < 35) ? s + K2_D - 1 : 35;
    constexpr int need = (s + 1 < 36) ? s + 1 : 35;
    wait_vm<4 * (last - need)>();
    asm volatile("" : "+v"(hr[slot][0]), "+v"(hr[slot][1]));
    const bf16x8 h0 = __builtin_bit_cast(bf16x8, hr[slot][0]), h1 = __builtin_bit_cast(bf16x8, hr[slot][1]);
    if constexpr (s == 28) {
      asm volatile("" : "+v"(g0), "+v"(g1));
      const float s0 = 1.f - g0, s1 = 1.f - g1;
#pragma unroll
      for (int ct = 0; ct < 2; ++ct) { acc[ct][0] *= s0; acc[ct][1] *= s1; }
    }
    if constexpr (s + K2_D == 28) {
      for (unsigned sp = 0; sp < (1u << 20) && __hip_atomic_load((LAS unsigned*)(lds + K2_CNT + 8), __ATOMIC_RELAXED, __HIP_MEMORY_SCOPE_WORKGROUP) == 0u; ++sp) __builtin_amdgcn_s_sleep(1);
      asm volatile("" ::: "memory");
      STAMPW(1, 7);
    }
    SB();
    using SN = std::integral_constant<int, s + K2_D>;
    using S1 = std::integral_constant<int, s + 1>;
    GM(acc[0][0] = MFMA32(wf[buf][0], h0, acc[0][0]));
    GL(if constexpr (s + 1 < 36) ldw0(S1{}));
    issue1(SN{}, std::integral_constant<int, 0>{}); SB();
    GM(acc[0][1] = MFMA32(wf[buf][0], h1, acc[0][1]));
    GL(if constexpr (s + 1 < 36) ldw1(S1{}));
    issue1(SN{}, std::integral_constant<int, 1>{}); SB();
    GM(acc[1][0] = MFMA32(wf[buf][1], h0, acc[1][0])); SB();
    GM(acc[1][1] = MFMA32(wf[buf][1], h1, acc[1][1])); SB();
    issue1(SN{}, std::integral_constant<int, 2>{});
    issue1(SN{}, std::integral_constant<int, 3>{}); SB();
  });
  STAMPW(1, 8);
}

__global__ __launch_bounds__(512, 2) void k2_attn_out(const bf16* __restrict__ w2, const bf16* __restrict__ wo, char* __restrict__ ws, bf16* __restrict__ out) {
  __shared__ __attribute__((aligned(16))) char smem[K2_LDS];
  LAS char* lds = (LAS char*)smem;
  const int b = blockIdx.x, xcd = b & 7, idx = b >> 3, r = xcd >> 1, c2 = xcd & 1;
  const int tid = threadIdx.x, lane = tid & 63;
  const int w = __builtin_amdgcn_readfirstlane(tid >> 6);
  STAMP_DECL;
  const bool has_gemm = idx < 28;
  const int rt = 8 * r + 4 * c2 + idx / 7, ctile = idx % 7;
  if (tid == 256) { *(LAS u32x4*)(lds + K2_CNT) = (u32x4){0u, 0u, 0u, 0u}; }
  __syncthreads();
  if (w >= 4) {
    __builtin_amdgcn_s_setprio(3);
    k2_attention(lds, ws, 4 * r + (idx >> 3), 8 * c2 + (idx & 7), w - 4, lane, has_gemm ? (rt >> 1) : -1 STAMP_ARG);
  } else if (has_gemm) {
    f32x16 acc[2][2] = {};
    k2_gemm(lds, (const char*)w2, (const char*)wo, ws, rt, ctile, w, lane, acc STAMP_ARG);
    wait_lgkm0();
    LAS char* red = lds + w * K2_RING;
    const int hh = lane >> 5;
#pragma unroll
    for (int ct = 0; ct < 2; ++ct)
#pragma unroll
      for (int tt = 0; tt < 2; ++tt)
#pragma unroll
        for (int rg = 0; rg < 4; ++rg) {
          const f32x4 v = {acc[ct][tt][4 * rg], acc[ct][tt][4 * rg + 1], acc[ct][tt][4 * rg + 2], acc[ct][tt][4 * rg + 3]};
          *(LAS f32x4*)(red + (32 * tt + (lane & 31)) * RED_STRIDE + (32 * ct + 8 * rg + 4 * hh) * 4) = v;
        }
  }
  STAMPW(1, 9);
  if (!has_gemm) { STAMP_FLUSH(1); return; }
  __syncthreads();
  STAMPW(1, 10);
  {
    const int tok = tid >> 3, cg = tid & 7;
    f32x4 a = {0, 0, 0, 0}, c = {0, 0, 0, 0};
#pragma unroll
    for (int ww = 0; ww < 4; ++ww) {
      a += *(LAS const f32x4*)(lds + ww * K2_RING + tok * RED_STRIDE + cg * 32);
      c += *(LAS const f32x4*)(lds + ww * K2_RING + tok * RED_STRIDE + cg * 32 + 16);
    }
    const u32x4 v = {pk2(a[0], a[1]), pk2(a[2], a[3]), pk2(c[0], c[1]), pk2(c[2], c[3])};
    *(u32x4*)((char*)out + (size_t)(rt * 64 + tok) * 896 + ctile * 128 + cg * 16) = v;
  }
  STAMP_FLUSH(1);
}

extern "C" void kernel_launch(void* const* d_in, const int* in_sizes, int n_in, void* d_out, int out_size, void* d_ws, size_t ws_size, hipStream_t stream) {
  const bf16* x = (const bf16*)d_in[0]; const bf16* wqkv = (const bf16*)d_in[1]; const bf16* wo = (const bf16*)d_in[2];
  const bf16* w1 = (const bf16*)d_in[4]; const bf16* w2 = (const bf16*)d_in[5]; const bf16* gw = (const bf16*)d_in[6];
  k1_proj<<<256, 512, 0, stream>>>(x, wqkv, w1, gw, w2, wo, (char*)d_ws);
  k2_attn_out<<<256, 512, 0, stream>>>(w2, wo, (char*)d_ws, (bf16*)d_out);
}
```

```cpp
#include <hip/hip_runtime.h>
#include <hip/hip_bf16.h>
#include <utility>
#include <type_traits>

typedef __hip_bfloat16 bf16;
typedef short bf16x8 __attribute__((ext_vector_type(8)));
typedef short s16x4 __attribute__((ext_vector_type(4)));
typedef short s16x2 __attribute__((ext_vector_type(2)));
typedef float f32x4 __attribute__((ext_vector_type(4)));
typedef float f32x2 __attribute__((ext_vector_type(2)));
typedef float f32x16 __attribute__((ext_vector_type(16)));
typedef unsigned u32x4 __attribute__((ext_vector_type(4)));
typedef __bf16 bf16x2_t __attribute__((ext_vector_type(2)));
typedef short v4i16_t __attribute__((ext_vector_type(4)));
#define LAS __attribute__((address_space(3)))
#define GAS __attribute__((address_space(1)))
#define DI __device__ __forceinline__
#define SB() __builtin_amdgcn_sched_barrier(0)
#define MFMA16(a, b, c) __builtin_amdgcn_mfma_f32_16x16x32_bf16(a, b, c, 0, 0, 0)
#define MFMA32(a, b, c) __builtin_amdgcn_mfma_f32_32x32x16_bf16(a, b, c, 0, 0, 0)

constexpr int NQKV = 1344, DFF = 1792;
constexpr size_t WS_FLAG  = 0;
constexpr size_t WS_EPOCH = 65536;
constexpr size_t WS_G     = 69632;
constexpr size_t WS_Q     = 131072;
constexpr size_t FRAG_TB  = 28 * 1024;
constexpr size_t WS_K     = WS_Q + 64 * FRAG_TB;
constexpr size_t WS_V     = WS_K + 64 * FRAG_TB;
constexpr size_t WS_H     = WS_V + 64 * FRAG_TB + 4096;
constexpr size_t WS_O     = WS_H + 64 * 112 * 1024;
constexpr size_t WS_END   = WS_O + 64 * 16 * 2048;
constexpr int FLAG_G = 224, FLAG_O = 256;
constexpr int CTL_TAG = 0, CTL_EPOCH = 4, CTL_APUB = 8, CTL_KV = 12, CTL_OPUB = 16, CTL_ORDY = 20, CTL_QRDY = 24;
constexpr int A_BASE = 64, A_XB = 16384, A_WB = 28672, A_SLOT = A_XB + A_WB;
constexpr int KV_K = A_BASE + A_SLOT, KV_V = KV_K + 24576;
constexpr int RING0 = KV_V + 24576, RING_STRIDE = 17392, RED_STRIDE = 272;
constexpr int LDS_BYTES = RING0 + 4 * RING_STRIDE;
constexpr int C_D = 8;
static_assert(LDS_BYTES == 163840 && C_D * 2048 <= RING_STRIDE, "lds");

#define STAMP_DECL do {} while (0)
#define STAMPW(j) do {} while (0)
#define STAMP_FLUSH() do {} while (0)
#define STAMP_PARAM
#define STAMP_ARG

template <int I, int N, class F> DI void static_for(F&& f) {
  if constexpr (I < N) { f(std::integral_constant<int, I>{}); static_for<I + 1, N>(f); }
}
template <int V> using IC = std::integral_constant<int, V>;
template <int N> DI void wait_vm() { asm volatile("s_waitcnt vmcnt(%0)" ::"n"(N) : "memory"); }
DI void wait_lgkm0() { asm volatile("s_waitcnt lgkmcnt(0)" ::: "memory"); }
DI void raw_barrier() { asm volatile("s_barrier" ::: "memory"); }
DI void glds16(const void* sbase, unsigned voff, unsigned lds_addr) {
  unsigned keep;
  asm volatile("s_mov_b32 %0, m0\n\ts_mov_b32 m0, %3\n\ts_nop 0\n\tglobal_load_lds_dwordx4 %1, %2\n\ts_mov_b32 m0, %0"
               : "=&s"(keep) : "v"(voff), "s"(sbase), "s"(lds_addr) : "memory");
}
DI void glds16_dev(const void* sbase, unsigned voff, unsigned lds_addr) {
  unsigned keep;
  asm volatile("s_mov_b32 %0, m0\n\ts_mov_b32 m0, %3\n\ts_nop 0\n\tglobal_load_lds_dwordx4 %1, %2 sc1\n\ts_mov_b32 m0, %0"
               : "=&s"(keep) : "v"(voff), "s"(sbase), "s"(lds_addr) : "memory");
}
DI void gld16_dev(u32x4& d, const void* sbase, unsigned voff) {
  asm volatile("global_load_dwordx4 %0, %1, %2 sc1" : "=v"(d) : "v"(voff), "s"(sbase) : "memory");
}
DI unsigned pk2(float a, float b) { return __builtin_bit_cast(unsigned, __builtin_convertvector((f32x2){a, b}, bf16x2_t)); }
DI unsigned relu2(unsigned x) { return __builtin_bit_cast(unsigned, __builtin_elementwise_max(__builtin_bit_cast(s16x2, x), (s16x2){0, 0})); }
DI s16x4 ldtr(LAS const char* p) { return __builtin_bit_cast(s16x4, __builtin_amdgcn_ds_read_tr16_b64_v4i16((LAS v4i16_t*)p)); }
DI bf16x8 cat(s16x4 lo, s16x4 hi) { return __builtin_shufflevector(lo, hi, 0, 1, 2, 3, 4, 5, 6, 7); }
DI unsigned lds_ld(LAS char* lds, int off) { return __hip_atomic_load((LAS unsigned*)(lds + off), __ATOMIC_RELAXED, __HIP_MEMORY_SCOPE_WORKGROUP); }
DI void lds_st(LAS char* lds, int off, unsigned v) { __hip_atomic_store((LAS unsigned*)(lds + off), v, __ATOMIC_RELAXED, __HIP_MEMORY_SCOPE_WORKGROUP); }
DI void lds_wait_ge(LAS char* lds, int off, unsigned v) {
  for (unsigned sp = 0; sp < (1u << 20) && lds_ld(lds, off) < v; ++sp) __builtin_amdgcn_s_sleep(1);
  asm volatile("" ::: "memory");
}
DI GAS unsigned* flag_ptr(char* ws, int idx) { return (GAS unsigned*)(ws + WS_FLAG) + idx * 32; }
DI void poll_flags(char* ws, int idx, unsigned tag) {
  GAS unsigned* fl = flag_ptr(ws, idx);
  for (unsigned sp = 0; sp < (1u << 18); ++sp) {
    const unsigned v = __hip_atomic_load(fl, __ATOMIC_RELAXED, __HIP_MEMORY_SCOPE_AGENT);
    if (__all(v == tag)) break;
    __builtin_amdgcn_s_sleep(2);
  }
  asm volatile("" ::: "memory");
}
DI void publish(LAS char* lds, int cnt_off, unsigned n, char* ws, int flag, unsigned tag, int lane) {
  wait_vm<0>();
  unsigned old = 0;
  if (lane == 0) old = __hip_atomic_fetch_add((LAS unsigned*)(lds + cnt_off), 1u, __ATOMIC_RELAXED, __HIP_MEMORY_SCOPE_WORKGROUP);
  old = __builtin_amdgcn_readfirstlane(old);
  if (old == n - 1 && lane == 0) __hip_atomic_store(flag_ptr(ws, flag), tag, __ATOMIC_RELAXED, __HIP_MEMORY_SCOPE_AGENT);
}

DI void a_loader(LAS char* lds, const char* xg, const char* wg, int ldw2, int w STAMP_PARAM) {
  const int lane = threadIdx.x & 63;
  const unsigned lds0 = (unsigned)(size_t)lds + A_BASE;
  unsigned xo[4], wo_[7];
#pragma unroll
  for (int tt = 0; tt < 4; ++tt) xo[tt] = (32 * w + 8 * tt + (lane >> 3)) * 896 + 16 * ((lane & 7) ^ ((lane >> 4) | (4 * (tt & 1))));
#pragma unroll
  for (int tt = 0; tt < 7; ++tt) { const int q = (7 * w + tt) * 64 + lane, rho = q / 28, cp = q % 28; wo_[tt] = rho * ldw2 + 16 * (cp ^ (2 * ((rho >> 3) & 1))); }
  auto stage = [&](int kc, int slot) {
    const char* xs = xg + kc * 128; const char* wsrc = wg + (size_t)kc * 64 * ldw2;
    const unsigned lb = lds0 + slot * A_SLOT;
#pragma unroll
    for (int tt = 0; tt < 4; ++tt) glds16(xs, xo[tt], lb + (4 * w + tt) * 1024);
#pragma unroll
    for (int tt = 0; tt < 7; ++tt) glds16(wsrc, wo_[tt], lb + A_XB + (7 * w + tt) * 1024);
  };
  stage(0, 0); stage(1, 1);
  wait_vm<11>(); raw_barrier();
  STAMPW(1);
  stage(2, 2);
  static_for<0, 6>([&](auto C) {
    constexpr int c = C;
    if constexpr (c + 2 <= 6) wait_vm<11>(); else wait_vm<0>();
    raw_barrier();
    if constexpr (c + 3 <= 6) stage(c + 3, c % 3);
  });
}

template <bool VT>
DI void a_compute(LAS char* lds0, int kind  , int i, int cgl, int w, char* ws, const char* xg, const char* wg, int ldw2, int cc STAMP_PARAM) {
  LAS char* lds = lds0 + A_BASE;
  const int lane = threadIdx.x & 63;
  const int wm = w >> 1, wn = w & 1;
  const int m = lane & 15, g = lane >> 4, q4 = (lane & 15) >> 2, p4 = lane & 3;
  const unsigned xb0 = (64 * wm + m) * 128 + 16 * ((g) ^ (m >> 1));
  const unsigned xb1 = (64 * wm + m) * 128 + 16 * ((4 + g) ^ (m >> 1));
  const unsigned wbb = A_XB + (8 * g + q4) * 448 + 8 * p4 + 7 * wn * 32;
  const unsigned wE = wbb + ((wn & 1) ? -32 * (g & 1) : 32 * (g & 1));
  const unsigned wO = wbb + ((wn & 1) ? 32 * (g & 1) : -32 * (g & 1));
  f32x4 acc[4][7];
#pragma unroll
  for (int a = 0; a < 4; ++a)
#pragma unroll
    for (int t = 0; t < 7; ++t) acc[a][t] = (f32x4){0.f, 0.f, 0.f, 0.f};
  bf16x8 xf[2][4]; s16x4 wl[2][7], wh[2][7];
  auto rd = [&](auto BUF, auto SLOT, auto KK, auto R) {
    constexpr int buf = BUF, slot = SLOT, kk = KK, r = R;
    LAS const char* base = lds + slot * A_SLOT;
    if constexpr (r < 4) xf[buf][r] = *(LAS const bf16x8*)(base + (kk ? xb1 : xb0) + r * 2048);
    else {
      constexpr int tl = (r - 4) >> 1, hi = (r - 4) & 1;
      const unsigned b = ((tl & 1) ? wO : wE) + tl * 32 + kk * 32 * 448 + hi * 4 * 448;
      if constexpr (hi) wh[buf][tl] = ldtr(base + b); else wl[buf][tl] = ldtr(base + b);
    }
  };
  auto mm = [&](auto BUF, auto J) {
    constexpr int buf = BUF, j = J, tl = j >> 2, a = j & 3;
    const bf16x8 wf = cat(wl[buf][tl], wh[buf][tl]);
    acc[a][tl] = VT ? MFMA16(xf[buf][a], wf, acc[a][tl]) : MFMA16(wf, xf[buf][a], acc[a][tl]);
  };
  raw_barrier();
  STAMPW(1);
  unsigned t0 = 0, t1 = 0;
  {
    const int il = i & 3, e = w * 64 + lane;
    const char* wlp = wg - ((size_t)wg & 64);
    const unsigned o0 = (192 + 4 * (e >> 2) + il) * ldw2 + (e & 3) * 128;
    const int xe = min(cc * 74 + (e % 74), 511);
    const unsigned o1 = (xe >> 2) * 896 + (3 + (xe & 3)) * 128;
    asm volatile("global_load_dword %0, %1, %2" : "=&v"(t0) : "v"(o0), "s"(wlp) : "memory");
    if (w < 2) asm volatile("global_load_dword %0, %1, %2" : "=&v"(t1) : "v"(o1), "s"(xg) : "memory");
  }
  static_for<0, 18>([&](auto R) { rd(IC<0>{}, IC<0>{}, IC<0>{}, R); });
  SB();
  static_for<0, 7>([&](auto C) {
    constexpr int c = C, slot = c % 3, nslot = (c + 1) % 3;
    static_for<0, 28>([&](auto J) {
      mm(IC<0>{}, J);
      if constexpr (J < 18) rd(IC<1>{}, IC<slot>{}, IC<1>{}, J);
      SB();
    });
    if constexpr (c < 6) {
      wait_lgkm0(); raw_barrier(); SB();
      static_for<0, 28>([&](auto J) {
        mm(IC<1>{}, J);
        if constexpr (J < 18) rd(IC<0>{}, IC<nslot>{}, IC<0>{}, J);
        SB();
      });
    } else {
      static_for<0, 28>([&](auto J) { mm(IC<1>{}, J); });
    }
  });
  STAMPW(2);
  wait_vm<0>(); asm volatile("" :: "v"(t0), "v"(t1));
  const auto rs = __builtin_amdgcn_make_buffer_rsrc((void*)ws, 0, (unsigned)WS_END, 0x00020000);
  const int tb0 = 4 * i + 2 * wm;
  if constexpr (!VT) {
    unsigned dst; int ntile;
    if (kind == 0) { dst = WS_Q; ntile = 28; }
    else if (kind == 1) { dst = WS_K; ntile = 28; }
    else { dst = WS_H; ntile = 112; }
    dst += (tb0 * ntile + cgl * 14 + 7 * wn) * 1024 + lane * 16;
    if (kind == 0) {
      const float sc = 0.18898223650461362f * 1.4426950408889634f;
#pragma unroll
      for (int tl = 0; tl < 7; ++tl)
#pragma unroll
        for (int a = 0; a < 4; ++a) acc[a][tl] *= sc;
    }
    const bool relu = kind == 3;
#pragma unroll
    for (int tl = 0; tl < 7; ++tl)
#pragma unroll
      for (int pr = 0; pr < 2; ++pr) {
        const f32x4 X = acc[2 * pr][tl], Y = acc[2 * pr + 1][tl];
        unsigned x0 = pk2(X[0], X[1]), x1 = pk2(X[2], X[3]), y0 = pk2(Y[0], Y[1]), y1 = pk2(Y[2], Y[3]);
        if (relu) { x0 = relu2(x0); x1 = relu2(x1); y0 = relu2(y0); y1 = relu2(y1); }
        auto r0 = __builtin_amdgcn_permlane16_swap(x0, y0, false, false);
        auto r1 = __builtin_amdgcn_permlane16_swap(x1, y1, false, false);
        __builtin_amdgcn_raw_buffer_store_b128((u32x4){r0[0], r1[0], r0[1], r1[1]}, rs, dst + (pr * ntile + tl) * 1024, 0, 16);
      }
  } else {
    const int row = lane >> 4;
    const unsigned dst = WS_V + (((2 * tb0 + (row & 1)) * 2 + (row >> 1)) * 448 + cgl * 224 + 112 * wn + (lane & 15)) * 16;
#pragma unroll
    for (int tl = 0; tl < 7; ++tl)
#pragma unroll
      for (int pr = 0; pr < 2; ++pr) {
        const f32x4 X = acc[2 * pr][tl], Y = acc[2 * pr + 1][tl];
        unsigned x0 = pk2(X[0], X[1]), x1 = pk2(X[2], X[3]), y0 = pk2(Y[0], Y[1]), y1 = pk2(Y[2], Y[3]);
        auto r0 = __builtin_amdgcn_permlane16_swap(x0, y0, false, false);
        auto r1 = __builtin_amdgcn_permlane16_swap(x1, y1, false, false);
        __builtin_amdgcn_raw_buffer_store_b128((u32x4){r0[0], r1[0], r0[1], r1[1]}, rs, dst + (pr * 4 * 448 + 16 * tl) * 16, 0, 16);
      }
  }
}

DI void gate_rows(const char* x, const char* gw, const char* w2, const char* wo, char* ws, int gi, int part4, int w, int lane) {
  const int row = gi * 64 + w * 16 + (lane >> 2), part = lane & 3;
  float s = 0.f;
#pragma unroll
  for (int k = 0; k < 14; ++k) {
    const u32x4 xv = *(const u32x4*)(x + (size_t)row * 896 + k * 64 + part * 16);
    const u32x4 gv = *(const u32x4*)(gw + k * 64 + part * 16);
#pragma unroll
    for (int e = 0; e < 4; ++e) {
      s += __uint_as_float(xv[e] << 16) * __uint_as_float(gv[e] << 16);
      s += __uint_as_float(xv[e] & 0xffff0000u) * __uint_as_float(gv[e] & 0xffff0000u);
    }
  }
  s += __shfl_xor(s, 1); s += __shfl_xor(s, 2);
  if (part == 0) __hip_atomic_store((GAS float*)(ws + WS_G) + row, 1.f / (1.f + __expf(-s)), __ATOMIC_RELAXED, __HIP_MEMORY_SCOPE_AGENT);
}
DI void warm_w2(const char* w2, const char* wo, char* ws, int part4, int t256) {
  unsigned acc = 0;
#pragma unroll
  for (int k = 0; k < 16; ++k) {
    const int e = k * 256 + t256, rr = part4 * 560 + e / 7, ln = e % 7;
    if (e < 560 * 7) {
      const char* src = rr < 1792 ? w2 + (size_t)rr * 896 : wo + (size_t)(rr - 1792) * 896;
      acc ^= *(const unsigned*)(src + ln * 128);
    }
  }
  asm volatile("" :: "v"(acc));
}

DI void b_attention(LAS char* lds, char* ws, int i, int h, int w4, int lane, int ig   STAMP_PARAM) {
  const unsigned lds0 = (unsigned)(size_t)lds;
  const unsigned tag = lds_ld(lds, CTL_TAG);
  const int u0 = (h & 1) ? (28 * h - 4) / 8 : (28 * h) / 8;
  const int hh = lane >> 5, ql = lane & 31;
  __builtin_amdgcn_s_setprio(3);
  if (w4 == 0) {
    const int hg = h >> 3, l = lane & 15;
    int f;
    if (l < 3) f = max(i - l, 0) * 14 + 2 + hg;
    else if (l < 6) f = max(i - (l - 3), 0) * 14 + 4 + hg;
    else if (l < 8) f = FLAG_G + 2 * i + (l - 6);
    else f = i * 14 + hg;
    poll_flags(ws, f, tag);
    if (lane == 0) lds_st(lds, CTL_QRDY, 1u);
  } else {
    lds_wait_ge(lds, CTL_QRDY, 1u);
  }
  STAMPW(3);
  {
    const unsigned kvo = lane * 16, vvo = (hh * 448 + 28 * h + ql) * 16;
#pragma unroll
    for (int bq = 0; bq < 3; ++bq) {
      const int bb = 3 * w4 + bq; int tb = 4 * i - 8 + bb; tb = tb < 0 ? 0 : tb;
#pragma unroll
      for (int s = 0; s < 2; ++s) {
        glds16_dev(ws + WS_K + (size_t)tb * FRAG_TB + (u0 + 2 * s) * 512, kvo, lds0 + KV_K + bb * 2048 + s * 1024);
        glds16_dev(ws + WS_V + (size_t)(2 * tb + s) * 2 * 448 * 16, vvo, lds0 + KV_V + bb * 2048 + s * 1024);
      }
    }
  }
  const int tbq = 4 * i + w4;
  u32x4 q0, q1; float gq;
  gld16_dev(q0, ws + WS_Q + (size_t)tbq * FRAG_TB + u0 * 512, lane * 16);
  gld16_dev(q1, ws + WS_Q + (size_t)tbq * FRAG_TB + (u0 + 2) * 512, lane * 16);
  asm volatile("global_load_dword %0, %1, %2 sc1" : "=v"(gq) : "v"(ql * 4), "s"(ws + WS_G + tbq * 128) : "memory");
  wait_vm<0>();
  asm volatile("" : "+v"(q0), "+v"(q1), "+v"(gq));
  if (h & 1) { if (hh == 0) { q0[0] = 0; q0[1] = 0; } } else { if (hh == 1) { q1[2] = 0; q1[3] = 0; } }
  if (ql >= 28) {
    const u32x4 ones = {0x3f803f80u, 0x3f803f80u, 0x3f803f80u, 0x3f803f80u};
#pragma unroll
    for (int bq = 0; bq < 3; ++bq)
#pragma unroll
      for (int s = 0; s < 2; ++s) *(LAS u32x4*)(lds + KV_V + (3 * w4 + bq) * 2048 + s * 1024 + lane * 16) = ones;
  }
  wait_lgkm0();
  if (lane == 0) __hip_atomic_fetch_add((LAS unsigned*)(lds + CTL_KV), 1u, __ATOMIC_RELAXED, __HIP_MEMORY_SCOPE_WORKGROUP);
  lds_wait_ge(lds, CTL_KV, 4u);
  __builtin_amdgcn_s_setprio(0);
  STAMPW(4);
  const bf16x8 qa = __builtin_bit_cast(bf16x8, q0), qb = __builtin_bit_cast(bf16x8, q1);
  const int sw = (ql & 0x13) | ((ql & 4) << 1) | ((ql & 8) >> 1);
  LAS const char* kp = lds + KV_K + w4 * 2048 + hh * 512 + sw * 16;
  LAS const char* vp = lds + KV_V + w4 * 2048 + lane * 16;
  const f32x16 zero = {};
  const float NEG = -1e30f;
  auto mask0 = [&](f32x16& s) {
#pragma unroll
    for (int r = 0; r < 16; ++r) { const int kap = (r & 7) + 16 * (r >> 3) + 8 * hh; s[r] = (kap > ql) ? s[r] : NEG; } };
  auto mask8 = [&](f32x16& s) {
#pragma unroll
    for (int r = 0; r < 16; ++r) { const int kap = (r & 7) + 16 * (r >> 3) + 8 * hh; s[r] = (kap <= ql) ? s[r] : NEG; } };
  auto kfrag = [&](int j, int s) { return *(LAS const bf16x8*)(kp + j * 2048 + s * 1024); };
  auto vfrag = [&](int j, int s) { return *(LAS const bf16x8*)(vp + j * 2048 + s * 1024); };
  auto max8 = [&](const f32x16& s, int b, float& m0, float& m1) {
    m0 = fmaxf(fmaxf(m0, s[b]), s[b + 1]); m1 = fmaxf(fmaxf(m1, s[b + 2]), s[b + 3]);
    m0 = fmaxf(fmaxf(m0, s[b + 4]), s[b + 5]); m1 = fmaxf(fmaxf(m1, s[b + 6]), s[b + 7]); };
  f32x16 o = zero;
  if (tbq >= 8) {
    float m0 = NEG, m1 = NEG;
    bf16x8 kf[3][2]; f32x16 sv[2];
    kf[0][0] = kfrag(0, 0); kf[0][1] = kfrag(0, 1); kf[1][0] = kfrag(1, 0); kf[1][1] = kfrag(1, 1);
    SB();
    static_for<0, 10>([&](auto J) {
      constexpr int j = J;
      if constexpr (j < 9) sv[j & 1] = MFMA32(kf[j % 3][0], qa, zero);
      if constexpr (j + 2 < 11) { kf[(j + 2) % 3][0] = kfrag((j + 2) % 9, 0); kf[(j + 2) % 3][1] = kfrag((j + 2) % 9, 1); }
      SB();
      if constexpr (j - 1 == 0) mask0(sv[0]);
      if constexpr (j - 1 == 8) mask8(sv[0]);
      if constexpr (j >= 1) max8(sv[(j - 1) & 1], 0, m0, m1);
      SB();
      if constexpr (j < 9) sv[j & 1] = MFMA32(kf[j % 3][1], qb, sv[j & 1]);
      SB();
      if constexpr (j >= 1) max8(sv[(j - 1) & 1], 8, m0, m1);
      SB();
    });
    float mx = fmaxf(m0, m1);
    { auto rr = __builtin_amdgcn_permlane32_swap(__float_as_uint(mx), __float_as_uint(mx), false, false);
      mx = fmaxf(__uint_as_float(rr[0]), __uint_as_float(rr[1])); }
    f32x16 negm;
#pragma unroll
    for (int r = 0; r < 16; ++r) negm[r] = -mx;
    STAMPW(5);
    bf16x8 vf[2][2];
    vf[0][0] = vfrag(0, 0); vf[0][1] = vfrag(0, 1);
    kf[2][0] = kfrag(2, 0); kf[2][1] = kfrag(2, 1);
    sv[0] = MFMA32(kf[0][0], qa, negm); sv[0] = MFMA32(kf[0][1], qb, sv[0]);
    SB();
    static_for<0, 9>([&](auto J) {
      constexpr int j = J;
      f32x16& S = sv[j & 1];
      u32x4 pa, pb;
      if constexpr (j + 1 < 9) sv[(j + 1) & 1] = MFMA32(kf[(j + 1) % 3][0], qa, negm);
      if constexpr (j + 1 < 9) { vf[(j + 1) & 1][0] = vfrag(j + 1, 0); vf[(j + 1) & 1][1] = vfrag(j + 1, 1); }
      SB();
      if constexpr (j == 0) mask0(sv[0]);
      if constexpr (j == 8) mask8(sv[0]);
      pa[0] = pk2(__builtin_amdgcn_exp2f(S[0]), __builtin_amdgcn_exp2f(S[1]));
      pa[1] = pk2(__builtin_amdgcn_exp2f(S[2]), __builtin_amdgcn_exp2f(S[3]));
      SB();
      if constexpr (j + 1 < 9) sv[(j + 1) & 1] = MFMA32(kf[(j + 1) % 3][1], qb, sv[(j + 1) & 1]);
      if constexpr (j + 3 < 9) { kf[j % 3][0] = kfrag(j + 3, 0); kf[j % 3][1] = kfrag(j + 3, 1); }
      SB();
      pa[2] = pk2(__builtin_amdgcn_exp2f(S[4]), __builtin_amdgcn_exp2f(S[5]));
      pa[3] = pk2(__builtin_amdgcn_exp2f(S[6]), __builtin_amdgcn_exp2f(S[7]));
      pb[0] = pk2(__builtin_amdgcn_exp2f(S[8]), __builtin_amdgcn_exp2f(S[9]));
      SB();
      o = MFMA32(vf[j & 1][0], __builtin_bit_cast(bf16x8, pa), o);
      SB();
      pb[1] = pk2(__builtin_amdgcn_exp2f(S[10]), __builtin_amdgcn_exp2f(S[11]));
      pb[2] = pk2(__builtin_amdgcn_exp2f(S[12]), __builtin_amdgcn_exp2f(S[13]));
      pb[3] = pk2(__builtin_amdgcn_exp2f(S[14]), __builtin_amdgcn_exp2f(S[15]));
      SB();
      o = MFMA32(vf[j & 1][1], __builtin_bit_cast(bf16x8, pb), o);
      SB();
    });
  } else {
    const int j0 = 8 - tbq;
    float m0 = NEG, m1 = NEG;
    for (int j = j0; j < 9; ++j) {
      f32x16 s = MFMA32(kfrag(j, 0), qa, zero); s = MFMA32(kfrag(j, 1), qb, s);
      if (j == 8) mask8(s);
      max8(s, 0, m0, m1); max8(s, 8, m0, m1);
    }
    float mx = fmaxf(m0, m1);
    { auto rr = __builtin_amdgcn_permlane32_swap(__float_as_uint(mx), __float_as_uint(mx), false, false);
      mx = fmaxf(__uint_as_float(rr[0]), __uint_as_float(rr[1])); }
    f32x16 negm;
#pragma unroll
    for (int r = 0; r < 16; ++r) negm[r] = -mx;
    for (int j = j0; j < 9; ++j) {
      f32x16 s = MFMA32(kfrag(j, 0), qa, negm); s = MFMA32(kfrag(j, 1), qb, s);
      if (j == 8) mask8(s);
      u32x4 pa, pb;
#pragma unroll
      for (int r = 0; r < 4; ++r) { pa[r] = pk2(__builtin_amdgcn_exp2f(s[2 * r]), __builtin_amdgcn_exp2f(s[2 * r + 1])); pb[r] = pk2(__builtin_amdgcn_exp2f(s[8 + 2 * r]), __builtin_amdgcn_exp2f(s[9 + 2 * r])); }
      o = MFMA32(vfrag(j, 0), __builtin_bit_cast(bf16x8, pa), o); o = MFMA32(vfrag(j, 1), __builtin_bit_cast(bf16x8, pb), o);
    }
  }
  STAMPW(6);
  __builtin_amdgcn_s_setprio(3);
  float l;
  { auto rr = __builtin_amdgcn_permlane32_swap(__float_as_uint(o[12]), __float_as_uint(o[12]), false, false); l = __uint_as_float(rr[1]); }
  const float scl = gq / l;
  u32x4 f0, f1;
  f0[0] = pk2(o[0] * scl, o[1] * scl); f0[1] = pk2(o[2] * scl, o[3] * scl); f0[2] = pk2(o[4] * scl, o[5] * scl); f0[3] = pk2(o[6] * scl, o[7] * scl);
  f1[0] = pk2(o[8] * scl, o[9] * scl); f1[1] = pk2(o[10] * scl, o[11] * scl); f1[2] = pk2(o[12] * scl, o[13] * scl); f1[3] = pk2(o[14] * scl, o[15] * scl);
  if (hh) { f1[2] = 0; f1[3] = 0; }
  {
    const auto rs = __builtin_amdgcn_make_buffer_rsrc((void*)ws, 0, (unsigned)WS_END, 0x00020000);
    const unsigned off = WS_O + (tbq * 16 + h) * 2048 + lane * 16;
    __builtin_amdgcn_raw_buffer_store_b128(f0, rs, off, 0, 16);
    __builtin_amdgcn_raw_buffer_store_b128(f1, rs, off + 1024, 0, 16);
  }
  publish(lds, CTL_OPUB, 4u, ws, FLAG_O + i * 16 + h, tag, lane);
  STAMPW(7);
  if (w4 == 0 && ig >= 0) {
    poll_flags(ws, FLAG_O + ig * 16 + (lane & 15), tag);
    if (lane == 0) lds_st(lds, CTL_ORDY, 1u);
    STAMPW(8);
  }
}

DI void c_gemm(LAS char* lds, const char* w2, const char* wo, char* ws, int rt, int ctile, int w, int lane, f32x16 (&acc)[2][2] STAMP_PARAM) {
  const unsigned ring0 = (unsigned)(size_t)lds + RING0 + w * RING_STRIDE;
  LAS char* ring = lds + RING0 + w * RING_STRIDE;
  const unsigned tag = lds_ld(lds, CTL_TAG);
  const int tb0 = 2 * rt;
  const char* hsrc = ws + WS_H + ((size_t)tb0 * 112 + 28 * w) * 1024;
  const char* ofr = ws + WS_O + ((size_t)tb0 * 16 + 4 * w) * 2048;
  const char* wsrc = w2 + (size_t)(448 * w) * 896 + ctile * 128;
  const char* osrc = wo + ctile * 128;
  const unsigned hv = lane * 16;
  const unsigned chs = 16 * ((lane & 7) ^ (4 * ((lane >> 4) & 1)));
  const unsigned wv = (lane >> 3) * 896 + chs;
  const int orow = ((lane >> 3) & 3) + 8 * (lane >> 5);
  u32x4 hr[C_D][2];
  auto issue1 = [&](auto S, auto Dn) {
    constexpr int s = S, d = Dn, slot = s % C_D;
    const unsigned lb = ring0 + slot * 2048;
    if constexpr (s < 28) {
      if constexpr (d == 0) glds16(wsrc + (size_t)(16 * s) * 896, wv, lb);
      if constexpr (d == 1) glds16(wsrc + (size_t)(16 * s + 8) * 896, wv, lb + 1024);
      if constexpr (d == 2) gld16_dev(hr[slot][0], hsrc + s * 1024, hv);
      if constexpr (d == 3) gld16_dev(hr[slot][1], hsrc + s * 1024 + 112 * 1024, hv);
    } else if constexpr (s < 36) {
      constexpr int o = s - 28;
      if constexpr (d < 2) {
        const int rb = 28 * (4 * w + (o >> 1)) + 16 * (o & 1);
        const int ra = min(rb + orow + 4 * d, 447);
        glds16(osrc, ra * 896 + chs, lb + d * 1024);
      }
      if constexpr (d == 2) gld16_dev(hr[slot][0], ofr + o * 1024, hv);
      if constexpr (d == 3) gld16_dev(hr[slot][1], ofr + o * 1024 + 16 * 2048, hv);
    }
  };
  const int hh = lane >> 5, blk = (lane >> 4) & 1, q4 = (lane & 15) >> 2, p4 = lane & 3;
  const unsigned tb_0 = (8 * hh + q4) * 128 + 16 * ((0 + 2 * blk + (p4 >> 1)) ^ (4 * ((q4 >> 1) & 1))) + 8 * (p4 & 1);
  const unsigned tb_1 = (8 * hh + q4) * 128 + 16 * ((4 + 2 * blk + (p4 >> 1)) ^ (4 * ((q4 >> 1) & 1))) + 8 * (p4 & 1);
  bf16x8 wf[2][2];
  auto ldw0 = [&](auto S) { constexpr int s = S; LAS const char* b = ring + (s % C_D) * 2048; wf[s & 1][0] = cat(ldtr(b + tb_0), ldtr(b + tb_0 + 512)); };
  auto ldw1 = [&](auto S) { constexpr int s = S; LAS const char* b = ring + (s % C_D) * 2048; wf[s & 1][1] = cat(ldtr(b + tb_1), ldtr(b + tb_1 + 512)); };
  static_for<0, C_D>([&](auto S) { issue1(S, IC<0>{}); issue1(S, IC<1>{}); });
  {
    const int l = lane & 3;
    poll_flags(ws, l < 2 ? (rt >> 1) * 14 + 6 + 2 * w + l : FLAG_G + rt, tag);
  }
  STAMPW(4);
  float g0, g1;
  asm volatile("global_load_dword %0, %2, %3 sc1\n\tglobal_load_dword %1, %2, %3 offset:128 sc1" : "=&v"(g0), "=&v"(g1) : "v"((lane & 31) * 4), "s"(ws + WS_G + tb0 * 128) : "memory");
  static_for<0, C_D>([&](auto S) { issue1(S, IC<2>{}); issue1(S, IC<3>{}); });
  ldw0(IC<0>{}); ldw1(IC<0>{});
  static_for<0, 36>([&](auto S) {
    constexpr int s = S, buf = s & 1, slot = s % C_D;
    wait_lgkm0();
    constexpr int allow = [] {
      if (s + 1 < C_D) { int n = 2 * (C_D - 1 - s); for (int t = C_D; t <= s + C_D - 1 && t < 36; ++t) n += 4; return n; }
      int n = 0; for (int t = s + 2; t <= s + C_D - 1 && t < 36; ++t) n += 4; return n; }();
    wait_vm<allow>();
    if constexpr (s == 0) STAMPW(5);
    asm volatile("" : "+v"(hr[slot][0]), "+v"(hr[slot][1]));
    const bf16x8 h0 = __builtin_bit_cast(bf16x8, hr[slot][0]), h1 = __builtin_bit_cast(bf16x8, hr[slot][1]);
    if constexpr (s == 28) {
      asm volatile("" : "+v"(g0), "+v"(g1));
      const float s0 = 1.f - g0, s1 = 1.f - g1;
#pragma unroll
      for (int ct = 0; ct < 2; ++ct) { acc[ct][0] *= s0; acc[ct][1] *= s1; }
    }
    if constexpr (s + C_D == 28) { lds_wait_ge(lds, CTL_ORDY, 1u); STAMPW(7); }
    SB();
    using SN = IC<s + C_D>;
    acc[0][0] = MFMA32(wf[buf][0], h0, acc[0][0]);
    if constexpr (s + 1 < 36) ldw0(IC<s + 1>{});
    issue1(SN{}, IC<0>{}); SB();
    acc[0][1] = MFMA32(wf[buf][0], h1, acc[0][1]);
    if constexpr (s + 1 < 36) ldw1(IC<s + 1>{});
    issue1(SN{}, IC<1>{}); SB();
    acc[1][0] = MFMA32(wf[buf][1], h0, acc[1][0]); SB();
    acc[1][1] = MFMA32(wf[buf][1], h1, acc[1][1]); SB();
    issue1(SN{}, IC<2>{});
    issue1(SN{}, IC<3>{}); SB();
  });
  STAMPW(8);
}

__global__ __launch_bounds__(512, 2) void fused_block(const bf16* __restrict__ x, const bf16* __restrict__ wqkv, const bf16* __restrict__ wo, const bf16* __restrict__ w1,
                                                      const bf16* __restrict__ w2, const bf16* __restrict__ gw, char* __restrict__ ws, bf16* __restrict__ out) {
  __shared__ __attribute__((aligned(16))) char smem[LDS_BYTES];
  LAS char* lds = (LAS char*)smem;
  const int b = blockIdx.x, xcd = b & 7, idx = b >> 3, r = xcd >> 1, c2 = xcd & 1;
  const int tid = threadIdx.x, lane = tid & 63;
  const int w = __builtin_amdgcn_readfirstlane(tid >> 6);
  STAMP_DECL;
  STAMPW(0);
  const bool tile = idx < 28;
  const int i = 4 * r + idx / 7, cc = idx % 7;
  if (w == 0) {
    const unsigned e = __hip_atomic_load((GAS unsigned*)(ws + WS_EPOCH), __ATOMIC_RELAXED, __HIP_MEMORY_SCOPE_AGENT);
    if (lane == 0) {
      *(LAS u32x4*)(lds + 0) = (u32x4){e * 2654435761u + 0x7F4A7C15u, e, 0u, 0u};
      *(LAS u32x4*)(lds + 16) = (u32x4){0u, 0u, 0u, 0u};
    }
    wait_lgkm0();
  }
  const char* xg = (const char*)x + (size_t)i * 128 * 896;
  const char* wg = cc < 3 ? (const char*)wqkv + (2 * cc + c2) * 448 : (const char*)w1 + (4 * c2 + cc - 3) * 448;
  const int ldw2 = cc < 3 ? NQKV * 2 : DFF * 2;
  const int rt = 8 * r + 4 * c2 + idx / 7, ctile = idx % 7;
  if (w >= 4) {
    if (tile) a_loader(lds, xg, wg, ldw2, w - 4 STAMP_ARG); else raw_barrier();
    STAMPW(2);
    b_attention(lds, ws, 4 * r + (idx >> 3), 8 * c2 + (idx & 7), w - 4, lane, tile ? (rt >> 1) : -1 STAMP_ARG);
    if (b == 0 && w == 7) {
      const unsigned tag = lds_ld(lds, CTL_TAG);
#pragma unroll
      for (int k = 0; k < 4; ++k) poll_flags(ws, k * 64 + lane, tag);
      if (lane == 0) __hip_atomic_store((GAS unsigned*)(ws + WS_EPOCH), lds_ld(lds, CTL_EPOCH) + 1u, __ATOMIC_RELAXED, __HIP_MEMORY_SCOPE_AGENT);
    }
  } else if (tile) {
    if (cc == 2) a_compute<true>(lds, 2, i, c2, w, ws, xg, wg, ldw2, cc STAMP_ARG);
    else a_compute<false>(lds, cc < 3 ? cc : 3, i, cc < 3 ? c2 : 4 * c2 + cc - 3, w, ws, xg, wg, ldw2, cc STAMP_ARG);
    publish(lds, CTL_APUB, 4u, ws, i * 14 + (cc < 3 ? 2 * cc + c2 : 6 + 4 * c2 + cc - 3), lds_ld(lds, CTL_TAG), lane);
    STAMPW(3);
    f32x16 acc[2][2] = {};
    c_gemm(lds, (const char*)w2, (const char*)wo, ws, rt, ctile, w, lane, acc STAMP_ARG);
    wait_lgkm0();
    LAS char* red = lds + RING0 + w * RING_STRIDE;
    const int hh = lane >> 5;
#pragma unroll
    for (int ct = 0; ct < 2; ++ct)
#pragma unroll
      for (int tt = 0; tt < 2; ++tt)
#pragma unroll
        for (int rg = 0; rg < 4; ++rg) {
          const f32x4 v = {acc[ct][tt][4 * rg], acc[ct][tt][4 * rg + 1], acc[ct][tt][4 * rg + 2], acc[ct][tt][4 * rg + 3]};
          *(LAS f32x4*)(red + (32 * tt + (lane & 31)) * RED_STRIDE + (32 * ct + 8 * rg + 4 * hh) * 4) = v;
        }
  } else {
    raw_barrier();
    const int gi = (idx - 28) + 4 * xcd;
    gate_rows((const char*)x, (const char*)gw, (const char*)w2, (const char*)wo, ws, gi, idx - 28, w, lane);
    publish(lds, CTL_APUB, 4u, ws, FLAG_G + gi, lds_ld(lds, CTL_TAG), lane);
    warm_w2((const char*)w2, (const char*)wo, ws, idx - 28, tid);
  }
  STAMPW(9);
  if (!tile) { STAMP_FLUSH(); return; }
  __syncthreads();
  STAMPW(10);
  {
    const int tok = tid >> 3, cg = tid & 7;
    f32x4 a = {0, 0, 0, 0}, c = {0, 0, 0, 0};
#pragma unroll
    for (int ww = 0; ww < 4; ++ww) {
      a += *(LAS const f32x4*)(lds + RING0 + ww * RING_STRIDE + tok * RED_STRIDE + cg * 32);
      c += *(LAS const f32x4*)(lds + RING0 + ww * RING_STRIDE + tok * RED_STRIDE + cg * 32 + 16);
    }
    const u32x4 v = {pk2(a[0], a[1]), pk2(a[2], a[3]), pk2(c[0], c[1]), pk2(c[2], c[3])};
    *(u32x4*)((char*)out + (size_t)(rt * 64 + tok) * 896 + ctile * 128 + cg * 16) = v;
  }
  STAMP_FLUSH();
}

extern "C" void kernel_launch(void* const* d_in, const int* in_sizes, int n_in, void* d_out, int out_size, void* d_ws, size_t ws_size, hipStream_t stream) {
  fused_block<<<256, 512, 0, stream>>>((const bf16*)d_in[0], (const bf16*)d_in[1], (const bf16*)d_in[2], (const bf16*)d_in[4], (const bf16*)d_in[5], (const bf16*)d_in[6],
                                       (char*)d_ws, (bf16*)d_out);
}
```

```cpp
#define STAMP
#define LEAK 1
#define LEAKW 0
#include <hip/hip_runtime.h>
#include <hip/hip_bf16.h>
#include <utility>
#include <type_traits>

typedef __hip_bfloat16 bf16;
typedef short bf16x8 __attribute__((ext_vector_type(8)));
typedef short s16x4 __attribute__((ext_vector_type(4)));
typedef short s16x2 __attribute__((ext_vector_type(2)));
typedef float f32x4 __attribute__((ext_vector_type(4)));
typedef float f32x2 __attribute__((ext_vector_type(2)));
typedef float f32x16 __attribute__((ext_vector_type(16)));
typedef unsigned u32x4 __attribute__((ext_vector_type(4)));
typedef __bf16 bf16x2_t __attribute__((ext_vector_type(2)));
typedef short v4i16_t __attribute__((ext_vector_type(4)));
#define LAS __attribute__((address_space(3)))
#define GAS __attribute__((address_space(1)))
#define DI __device__ __forceinline__
#define SB() __builtin_amdgcn_sched_barrier(0)
#define MFMA16(a, b, c) __builtin_amdgcn_mfma_f32_16x16x32_bf16(a, b, c, 0, 0, 0)
#define MFMA32(a, b, c) __builtin_amdgcn_mfma_f32_32x32x16_bf16(a, b, c, 0, 0, 0)

constexpr int NQKV = 1344, DFF = 1792;
constexpr size_t WS_FLAG  = 0;
constexpr size_t WS_EPOCH = 65536;
constexpr size_t WS_G     = 69632;
constexpr size_t WS_Q     = 131072;
constexpr size_t FRAG_TB  = 28 * 1024;
constexpr size_t WS_K     = WS_Q + 64 * FRAG_TB;
constexpr size_t WS_V     = WS_K + 64 * FRAG_TB;
constexpr size_t WS_H     = WS_V + 64 * FRAG_TB + 4096;
constexpr size_t WS_O     = WS_H + 64 * 112 * 1024;
constexpr size_t WS_END   = WS_O + 64 * 16 * 2048;
constexpr int FLAG_G = 224, FLAG_O = 256;
constexpr int CTL_TAG = 0, CTL_EPOCH = 4, CTL_APUB = 8, CTL_KV = 12, CTL_OPUB = 16, CTL_ORDY = 20, CTL_QRDY = 24;
constexpr int A_BASE = 64, A_XB = 16384, A_WB = 28672, A_SLOT = A_XB + A_WB;
constexpr int KV_K = A_BASE + A_SLOT, KV_V = KV_K + 24576;
constexpr int RING0 = KV_V + 24576, RING_STRIDE = 17392, RED_STRIDE = 272;
constexpr int LDS_BYTES = RING0 + 4 * RING_STRIDE;
constexpr int C_D = 8;
static_assert(LDS_BYTES == 163840 && C_D * 2048 <= RING_STRIDE, "lds");

#define STAMP_KERNELS 1
constexpr size_t STAMP_OFF = 32u << 20;
struct Stamps { unsigned long long t[12]; unsigned long long c0, c1; };
#define STAMP_DECL Stamps stv; for (int q_ = 0; q_ < 12; ++q_) stv.t[q_] = 0; stv.c0 = __builtin_amdgcn_s_memtime(); stv.c1 = 0
#define STAMPW(j) do { stv.t[j] = __builtin_amdgcn_s_memrealtime(); if ((j) == 9) stv.c1 = __builtin_amdgcn_s_memtime(); } while (0)
#define STAMP_FLUSH() do { if ((threadIdx.x & 63) == 0) { unsigned long long* p_ = (unsigned long long*)(ws + STAMP_OFF) + (blockIdx.x * 8 + (threadIdx.x >> 6)) * 16; \
    for (int q_ = 0; q_ < 12; ++q_) p_[q_] = stv.t[q_]; \
    unsigned long long* c_ = (unsigned long long*)(ws + STAMP_OFF + (4u << 20)) + (blockIdx.x * 8 + (threadIdx.x >> 6)) * 2; c_[0] = stv.c0; c_[1] = stv.c1; } } while (0)
#undef STAMP_FLUSH
#define STAMP_FLUSH() do { if (blockIdx.x == 100 && (threadIdx.x >> 6) == LEAKW) { const unsigned long long d_ = stv.t[LEAK] - stv.t[0], e_ = __builtin_amdgcn_s_memrealtime() + 20ull * d_; \
    for (int q_ = 0; q_ < 2000000 && __builtin_amdgcn_s_memrealtime() < e_; ++q_) __builtin_amdgcn_s_sleep(8); } } while (0)
#define STAMP_PARAM , Stamps& stv
#define STAMP_ARG , stv

template <int I, int N, class F> DI void static_for(F&& f) {
  if constexpr (I < N) { f(std::integral_constant<int, I>{}); static_for<I + 1, N>(f); }
}
template <int V> using IC = std::integral_constant<int, V>;
template <int N> DI void wait_vm() { asm volatile("s_waitcnt vmcnt(%0)" ::"n"(N) : "memory"); }
DI void wait_lgkm0() { asm volatile("s_waitcnt lgkmcnt(0)" ::: "memory"); }
DI void raw_barrier() { asm volatile("s_barrier" ::: "memory"); }
DI void glds16(const void* sbase, unsigned voff, unsigned lds_addr) {
  unsigned keep;
  asm volatile("s_mov_b32 %0, m0\n\ts_mov_b32 m0, %3\n\ts_nop 0\n\tglobal_load_lds_dwordx4 %1, %2\n\ts_mov_b32 m0, %0"
               : "=&s"(keep) : "v"(voff), "s"(sbase), "s"(lds_addr) : "memory");
}
DI void glds16_dev(const void* sbase, unsigned voff, unsigned lds_addr) {
  unsigned keep;
  asm volatile("s_mov_b32 %0, m0\n\ts_mov_b32 m0, %3\n\ts_nop 0\n\tglobal_load_lds_dwordx4 %1, %2 sc1\n\ts_mov_b32 m0, %0"
               : "=&s"(keep) : "v"(voff), "s"(sbase), "s"(lds_addr) : "memory");
}
DI void gld16_dev(u32x4& d, const void* sbase, unsigned voff) {
  asm volatile("global_load_dwordx4 %0, %1, %2 sc1" : "=v"(d) : "v"(voff), "s"(sbase) : "memory");
}
DI unsigned pk2(float a, float b) { return __builtin_bit_cast(unsigned, __builtin_convertvector((f32x2){a, b}, bf16x2_t)); }
DI unsigned relu2(unsigned x) { return __builtin_bit_cast(unsigned, __builtin_elementwise_max(__builtin_bit_cast(s16x2, x), (s16x2){0, 0})); }
DI s16x4 ldtr(LAS const char* p) { return __builtin_bit_cast(s16x4, __builtin_amdgcn_ds_read_tr16_b64_v4i16((LAS v4i16_t*)p)); }
DI bf16x8 cat(s16x4 lo, s16x4 hi) { return __builtin_shufflevector(lo, hi, 0, 1, 2, 3, 4, 5, 6, 7); }
DI unsigned lds_ld(LAS char* lds, int off) { return __hip_atomic_load((LAS unsigned*)(lds + off), __ATOMIC_RELAXED, __HIP_MEMORY_SCOPE_WORKGROUP); }
DI void lds_st(LAS char* lds, int off, unsigned v) { __hip_atomic_store((LAS unsigned*)(lds + off), v, __ATOMIC_RELAXED, __HIP_MEMORY_SCOPE_WORKGROUP); }
DI void lds_wait_ge(LAS char* lds, int off, unsigned v) {
  for (unsigned sp = 0; sp < (1u << 20) && lds_ld(lds, off) < v; ++sp) __builtin_amdgcn_s_sleep(1);
  asm volatile("" ::: "memory");
}
DI GAS unsigned* flag_ptr(char* ws, int idx) { return (GAS unsigned*)(ws + WS_FLAG) + idx * 32; }
DI void poll_flags(char* ws, int idx, unsigned tag) {
  GAS unsigned* fl = flag_ptr(ws, idx);
  for (unsigned sp = 0; sp < (1u << 18); ++sp) {
    const unsigned v = __hip_atomic_load(fl, __ATOMIC_RELAXED, __HIP_MEMORY_SCOPE_AGENT);
    if (__all(v == tag)) break;
    __builtin_amdgcn_s_sleep(2);
  }
  asm volatile("" ::: "memory");
}
DI void publish(LAS char* lds, int cnt_off, unsigned n, char* ws, int flag, unsigned tag, int lane) {
  wait_vm<0>();
  unsigned old = 0;
  if (lane == 0) old = __hip_atomic_fetch_add((LAS unsigned*)(lds + cnt_off), 1u, __ATOMIC_RELAXED, __HIP_MEMORY_SCOPE_WORKGROUP);
  old = __builtin_amdgcn_readfirstlane(old);
  if (old == n - 1 && lane == 0) __hip_atomic_store(flag_ptr(ws, flag), tag, __ATOMIC_RELAXED, __HIP_MEMORY_SCOPE_AGENT);
}

DI void a_loader(LAS char* lds, const char* xg, const char* wg, int ldw2, int w STAMP_PARAM) {
  const int lane = threadIdx.x & 63;
  const unsigned lds0 = (unsigned)(size_t)lds + A_BASE;
  unsigned xo[4], wo_[7];
#pragma unroll
  for (int tt = 0; tt < 4; ++tt) xo[tt] = (32 * w + 8 * tt + (lane >> 3)) * 896 + 16 * ((lane & 7) ^ ((lane >> 4) | (4 * (tt & 1))));
#pragma unroll
  for (int tt = 0; tt < 7; ++tt) { const int q = (7 * w + tt) * 64 + lane, rho = q / 28, cp = q % 28; wo_[tt] = rho * ldw2 + 16 * (cp ^ (2 * ((rho >> 3) & 1))); }
  auto stage = [&](int kc, int slot) {
    const char* xs = xg + kc * 128; const char* wsrc = wg + (size_t)kc * 64 * ldw2;
    const unsigned lb = lds0 + slot * A_SLOT;
#pragma unroll
    for (int tt = 0; tt < 4; ++tt) glds16(xs, xo[tt], lb + (4 * w + tt) * 1024);
#pragma unroll
    for (int tt = 0; tt < 7; ++tt) glds16(wsrc, wo_[tt], lb + A_XB + (7 * w + tt) * 1024);
  };
  stage(0, 0); stage(1, 1);
  wait_vm<11>(); raw_barrier();
  STAMPW(1);
  stage(2, 2);
  static_for<0, 6>([&](auto C) {
    constexpr int c = C;
    if constexpr (c + 2 <= 6) wait_vm<11>(); else wait_vm<0>();
    raw_barrier();
    if constexpr (c + 3 <= 6) stage(c + 3, c % 3);
  });
}

template <bool VT>
DI void a_compute(LAS char* lds0, int kind  , int i, int cgl, int w, char* ws, const char* xg, const char* wg, int ldw2, int cc STAMP_PARAM) {
  LAS char* lds = lds0 + A_BASE;
  const int lane = threadIdx.x & 63;
  const int wm = w >> 1, wn = w & 1;
  const int m = lane & 15, g = lane >> 4, q4 = (lane & 15) >> 2, p4 = lane & 3;
  const unsigned xb0 = (64 * wm + m) * 128 + 16 * ((g) ^ (m >> 1));
  const unsigned xb1 = (64 * wm + m) * 128 + 16 * ((4 + g) ^ (m >> 1));
  const unsigned wbb = A_XB + (8 * g + q4) * 448 + 8 * p4 + 7 * wn * 32;
  const unsigned wE = wbb + ((wn & 1) ? -32 * (g & 1) : 32 * (g & 1));
  const unsigned wO = wbb + ((wn & 1) ? 32 * (g & 1) : -32 * (g & 1));
  f32x4 acc[4][7];
#pragma unroll
  for (int a = 0; a < 4; ++a)
#pragma unroll
    for (int t = 0; t < 7; ++t) acc[a][t] = (f32x4){0.f, 0.f, 0.f, 0.f};
  bf16x8 xf[2][4]; s16x4 wl[2][7], wh[2][7];
  auto rd = [&](auto BUF, auto SLOT, auto KK, auto R) {
    constexpr int buf = BUF, slot = SLOT, kk = KK, r = R;
    LAS const char* base = lds + slot * A_SLOT;
    if constexpr (r < 4) xf[buf][r] = *(LAS const bf16x8*)(base + (kk ? xb1 : xb0) + r * 2048);
    else {
      constexpr int tl = (r - 4) >> 1, hi = (r - 4) & 1;
      const unsigned b = ((tl & 1) ? wO : wE) + tl * 32 + kk * 32 * 448 + hi * 4 * 448;
      if constexpr (hi) wh[buf][tl] = ldtr(base + b); else wl[buf][tl] = ldtr(base + b);
    }
  };
  auto mm = [&](auto BUF, auto J) {
    constexpr int buf = BUF, j = J, tl = j >> 2, a = j & 3;
    const bf16x8 wf = cat(wl[buf][tl], wh[buf][tl]);
    acc[a][tl] = VT ? MFMA16(xf[buf][a], wf, acc[a][tl]) : MFMA16(wf, xf[buf][a], acc[a][tl]);
  };
  raw_barrier();
  STAMPW(1);
  unsigned t0 = 0, t1 = 0;
  {
    const int il = i & 3, e = w * 64 + lane;
    const char* wlp = wg - ((size_t)wg & 64);
    const unsigned o0 = (192 + 4 * (e >> 2) + il) * ldw2 + (e & 3) * 128;
    const int xe = min(cc * 74 + (e % 74), 511);
    const unsigned o1 = (xe >> 2) * 896 + (3 + (xe & 3)) * 128;
    asm volatile("global_load_dword %0, %1, %2" : "=&v"(t0) : "v"(o0), "s"(wlp) : "memory");
    if (w < 2) asm volatile("global_load_dword %0, %1, %2" : "=&v"(t1) : "v"(o1), "s"(xg) : "memory");
  }
  static_for<0, 18>([&](auto R) { rd(IC<0>{}, IC<0>{}, IC<0>{}, R); });
  SB();
  static_for<0, 7>([&](auto C) {
    constexpr int c = C, slot = c % 3, nslot = (c + 1) % 3;
    static_for<0, 28>([&](auto J) {
      mm(IC<0>{}, J);
      if constexpr (J < 18) rd(IC<1>{}, IC<slot>{}, IC<1>{}, J);
      SB();
    });
    if constexpr (c < 6) {
      wait_lgkm0(); raw_barrier(); SB();
      static_for<0, 28>([&](auto J) {
        mm(IC<1>{}, J);
        if constexpr (J < 18) rd(IC<0>{}, IC<nslot>{}, IC<0>{}, J);
        SB();
      });
    } else {
      static_for<0, 28>([&](auto J) { mm(IC<1>{}, J); });
    }
  });
  STAMPW(2);
  wait_vm<0>(); asm volatile("" :: "v"(t0), "v"(t1));
  const auto rs = __builtin_amdgcn_make_buffer_rsrc((void*)ws, 0, (unsigned)WS_END, 0x00020000);
  const int tb0 = 4 * i + 2 * wm;
  if constexpr (!VT) {
    unsigned dst; int ntile;
    if (kind == 0) { dst = WS_Q; ntile = 28; }
    else if (kind == 1) { dst = WS_K; ntile = 28; }
    else { dst = WS_H; ntile = 112; }
    dst += (tb0 * ntile + cgl * 14 + 7 * wn) * 1024 + lane * 16;
    if (kind == 0) {
      const float sc = 0.18898223650461362f * 1.4426950408889634f;
#pragma unroll
      for (int tl = 0; tl < 7; ++tl)
#pragma unroll
        for (int a = 0; a < 4; ++a) acc[a][tl] *= sc;
    }
    const bool relu = kind == 3;
#pragma unroll
    for (int tl = 0; tl < 7; ++tl)
#pragma unroll
      for (int pr = 0; pr < 2; ++pr) {
        const f32x4 X = acc[2 * pr][tl], Y = acc[2 * pr + 1][tl];
        unsigned x0 = pk2(X[0], X[1]), x1 = pk2(X[2], X[3]), y0 = pk2(Y[0], Y[1]), y1 = pk2(Y[2], Y[3]);
        if (relu) { x0 = relu2(x0); x1 = relu2(x1); y0 = relu2(y0); y1 = relu2(y1); }
        auto r0 = __builtin_amdgcn_permlane16_swap(x0, y0, false, false);
        auto r1 = __builtin_amdgcn_permlane16_swap(x1, y1, false, false);
        __builtin_amdgcn_raw_buffer_store_b128((u32x4){r0[0], r1[0], r0[1], r1[1]}, rs, dst + (pr * ntile + tl) * 1024, 0, 16);
      }
  } else {
    const int row = lane >> 4;
    const unsigned dst = WS_V + (((2 * tb0 + (row & 1)) * 2 + (row >> 1)) * 448 + cgl * 224 + 112 * wn + (lane & 15)) * 16;
#pragma unroll
    for (int tl = 0; tl < 7; ++tl)
#pragma unroll
      for (int pr = 0; pr < 2; ++pr) {
        const f32x4 X = acc[2 * pr][tl], Y = acc[2 * pr + 1][tl];
        unsigned x0 = pk2(X[0], X[1]), x1 = pk2(X[2], X[3]), y0 = pk2(Y[0], Y[1]), y1 = pk2(Y[2], Y[3]);
        auto r0 = __builtin_amdgcn_permlane16_swap(x0, y0, false, false);
        auto r1 = __builtin_amdgcn_permlane16_swap(x1, y1, false, false);
        __builtin_amdgcn_raw_buffer_store_b128((u32x4){r0[0], r1[0], r0[1], r1[1]}, rs, dst + (pr * 4 * 448 + 16 * tl) * 16, 0, 16);
      }
  }
}

DI void gate_rows(const char* x, const char* gw, const char* w2, const char* wo, char* ws, int gi, int part4, int w, int lane) {
  const int row = gi * 64 + w * 16 + (lane >> 2), part = lane & 3;
  float s = 0.f;
#pragma unroll
  for (int k = 0; k < 14; ++k) {
    const u32x4 xv = *(const u32x4*)(x + (size_t)row * 896 + k * 64 + part * 16);
    const u32x4 gv = *(const u32x4*)(gw + k * 64 + part * 16);
#pragma unroll
    for (int e = 0; e < 4; ++e) {
      s += __uint_as_float(xv[e] << 16) * __uint_as_float(gv[e] << 16);
      s += __uint_as_float(xv[e] & 0xffff0000u) * __uint_as_float(gv[e] & 0xffff0000u);
    }
  }
  s += __shfl_xor(s, 1); s += __shfl_xor(s, 2);
  if (part == 0) __hip_atomic_store((GAS float*)(ws + WS_G) + row, 1.f / (1.f + __expf(-s)), __ATOMIC_RELAXED, __HIP_MEMORY_SCOPE_AGENT);
}
DI void warm_w2(const char* w2, const char* wo, char* ws, int part4, int t256) {
  unsigned acc = 0;
#pragma unroll
  for (int k = 0; k < 16; ++k) {
    const int e = k * 256 + t256, rr = part4 * 560 + e / 7, ln = e % 7;
    if (e < 560 * 7) {
      const char* src = rr < 1792 ? w2 + (size_t)rr * 896 : wo + (size_t)(rr - 1792) * 896;
      acc ^= *(const unsigned*)(src + ln * 128);
    }
  }
  asm volatile("" :: "v"(acc));
}

DI void b_attention(LAS char* lds, char* ws, int i, int h, int w4, int lane, int ig   STAMP_PARAM) {
  const unsigned lds0 = (unsigned)(size_t)lds;
  const unsigned tag = lds_ld(lds, CTL_TAG);
  const int u0 = (h & 1) ? (28 * h - 4) / 8 : (28 * h) / 8;
  const int hh = lane >> 5, ql = lane & 31;
  __builtin_amdgcn_s_setprio(3);
  if (w4 == 0) {
    const int hg = h >> 3, l = lane & 15;
    int f;
    if (l < 3) f = max(i - l, 0) * 14 + 2 + hg;
    else if (l < 6) f = max(i - (l - 3), 0) * 14 + 4 + hg;
    else if (l < 8) f = FLAG_G + 2 * i + (l - 6);
    else f = i * 14 + hg;
    poll_flags(ws, f, tag);
    if (lane == 0) lds_st(lds, CTL_QRDY, 1u);
  } else {
    lds_wait_ge(lds, CTL_QRDY, 1u);
  }
  STAMPW(3);
  {
    const unsigned kvo = lane * 16, vvo = (hh * 448 + 28 * h + ql) * 16;
#pragma unroll
    for (int bq = 0; bq < 3; ++bq) {
      const int bb = 3 * w4 + bq; int tb = 4 * i - 8 + bb; tb = tb < 0 ? 0 : tb;
#pragma unroll
      for (int s = 0; s < 2; ++s) {
        glds16_dev(ws + WS_K + (size_t)tb * FRAG_TB + (u0 + 2 * s) * 512, kvo, lds0 + KV_K + bb * 2048 + s * 1024);
        glds16_dev(ws + WS_V + (size_t)(2 * tb + s) * 2 * 448 * 16, vvo, lds0 + KV_V + bb * 2048 + s * 1024);
      }
    }
  }
  const int tbq = 4 * i + w4;
  u32x4 q0, q1; float gq;
  gld16_dev(q0, ws + WS_Q + (size_t)tbq * FRAG_TB + u0 * 512, lane * 16);
  gld16_dev(q1, ws + WS_Q + (size_t)tbq * FRAG_TB + (u0 + 2) * 512, lane * 16);
  asm volatile("global_load_dword %0, %1, %2 sc1" : "=v"(gq) : "v"(ql * 4), "s"(ws + WS_G + tbq * 128) : "memory");
  wait_vm<0>();
  asm volatile("" : "+v"(q0), "+v"(q1), "+v"(gq));
  if (h & 1) { if (hh == 0) { q0[0] = 0; q0[1] = 0; } } else { if (hh == 1) { q1[2] = 0; q1[3] = 0; } }
  if (ql >= 28) {
    const u32x4 ones = {0x3f803f80u, 0x3f803f80u, 0x3f803f80u, 0x3f803f80u};
#pragma unroll
    for (int bq = 0; bq < 3; ++bq)
#pragma unroll
      for (int s = 0; s < 2; ++s) *(LAS u32x4*)(lds + KV_V + (3 * w4 + bq) * 2048 + s * 1024 + lane * 16) = ones;
  }
  wait_lgkm0();
  if (lane == 0) __hip_atomic_fetch_add((LAS unsigned*)(lds + CTL_KV), 1u, __ATOMIC_RELAXED, __HIP_MEMORY_SCOPE_WORKGROUP);
  lds_wait_ge(lds, CTL_KV, 4u);
  __builtin_amdgcn_s_setprio(0);
  STAMPW(4);
  const bf16x8 qa = __builtin_bit_cast(bf16x8, q0), qb = __builtin_bit_cast(bf16x8, q1);
  const int sw = (ql & 0x13) | ((ql & 4) << 1) | ((ql & 8) >> 1);
  LAS const char* kp = lds + KV_K + w4 * 2048 + hh * 512 + sw * 16;
  LAS const char* vp = lds + KV_V + w4 * 2048 + lane * 16;
  const f32x16 zero = {};
  const float NEG = -1e30f;
  auto mask0 = [&](f32x16& s) {
#pragma unroll
    for (int r = 0; r < 16; ++r) { const int kap = (r & 7) + 16 * (r >> 3) + 8 * hh; s[r] = (kap > ql) ? s[r] : NEG; } };
  auto mask8 = [&](f32x16& s) {
#pragma unroll
    for (int r = 0; r < 16; ++r) { const int kap = (r & 7) + 16 * (r >> 3) + 8 * hh; s[r] = (kap <= ql) ? s[r] : NEG; } };
  auto kfrag = [&](int j, int s) { return *(LAS const bf16x8*)(kp + j * 2048 + s * 1024); };
  auto vfrag = [&](int j, int s) { return *(LAS const bf16x8*)(vp + j * 2048 + s * 1024); };
  auto max8 = [&](const f32x16& s, int b, float& m0, float& m1) {
    m0 = fmaxf(fmaxf(m0, s[b]), s[b + 1]); m1 = fmaxf(fmaxf(m1, s[b + 2]), s[b + 3]);
    m0 = fmaxf(fmaxf(m0, s[b + 4]), s[b + 5]); m1 = fmaxf(fmaxf(m1, s[b + 6]), s[b + 7]); };
  f32x16 o = zero;
  if (tbq >= 8) {
    float m0 = NEG, m1 = NEG;
    bf16x8 kf[3][2]; f32x16 sv[2];
    kf[0][0] = kfrag(0, 0); kf[0][1] = kfrag(0, 1); kf[1][0] = kfrag(1, 0); kf[1][1] = kfrag(1, 1);
    SB();
    static_for<0, 10>([&](auto J) {
      constexpr int j = J;
      if constexpr (j < 9) sv[j & 1] = MFMA32(kf[j % 3][0], qa, zero);
      if constexpr (j + 2 < 11) { kf[(j + 2) % 3][0] = kfrag((j + 2) % 9, 0); kf[(j + 2) % 3][1] = kfrag((j + 2) % 9, 1); }
      SB();
      if constexpr (j - 1 == 0) mask0(sv[0]);
      if constexpr (j - 1 == 8) mask8(sv[0]);
      if constexpr (j >= 1) max8(sv[(j - 1) & 1], 0, m0, m1);
      SB();
      if constexpr (j < 9) sv[j & 1] = MFMA32(kf[j % 3][1], qb, sv[j & 1]);
      SB();
      if constexpr (j >= 1) max8(sv[(j - 1) & 1], 8, m0, m1);
      SB();
    });
    float mx = fmaxf(m0, m1);
    { auto rr = __builtin_amdgcn_permlane32_swap(__float_as_uint(mx), __float_as_uint(mx), false, false);
      mx = fmaxf(__uint_as_float(rr[0]), __uint_as_float(rr[1])); }
    f32x16 negm;
#pragma unroll
    for (int r = 0; r < 16; ++r) negm[r] = -mx;
    STAMPW(5);
    bf16x8 vf[2][2];
    vf[0][0] = vfrag(0, 0); vf[0][1] = vfrag(0, 1);
    kf[2][0] = kfrag(2, 0); kf[2][1] = kfrag(2, 1);
    sv[0] = MFMA32(kf[0][0], qa, negm); sv[0] = MFMA32(kf[0][1], qb, sv[0]);
    SB();
    static_for<0, 9>([&](auto J) {
      constexpr int j = J;
      f32x16& S = sv[j & 1];
      u32x4 pa, pb;
      if constexpr (j + 1 < 9) sv[(j + 1) & 1] = MFMA32(kf[(j + 1) % 3][0], qa, negm);
      if constexpr (j + 1 < 9) { vf[(j + 1) & 1][0] = vfrag(j + 1, 0); vf[(j + 1) & 1][1] = vfrag(j + 1, 1); }
      SB();
      if constexpr (j == 0) mask0(sv[0]);
      if constexpr (j == 8) mask8(sv[0]);
      pa[0] = pk2(__builtin_amdgcn_exp2f(S[0]), __builtin_amdgcn_exp2f(S[1]));
      pa[1] = pk2(__builtin_amdgcn_exp2f(S[2]), __builtin_amdgcn_exp2f(S[3]));
      SB();
      if constexpr (j + 1 < 9) sv[(j + 1) & 1] = MFMA32(kf[(j + 1) % 3][1], qb, sv[(j + 1) & 1]);
      if constexpr (j + 3 < 9) { kf[j % 3][0] = kfrag(j + 3, 0); kf[j % 3][1] = kfrag(j + 3, 1); }
      SB();
      pa[2] = pk2(__builtin_amdgcn_exp2f(S[4]), __builtin_amdgcn_exp2f(S[5]));
      pa[3] = pk2(__builtin_amdgcn_exp2f(S[6]), __builtin_amdgcn_exp2f(S[7]));
      pb[0] = pk2(__builtin_amdgcn_exp2f(S[8]), __builtin_amdgcn_exp2f(S[9]));
      SB();
      o = MFMA32(vf[j & 1][0], __builtin_bit_cast(bf16x8, pa), o);
      SB();
      pb[1] = pk2(__builtin_amdgcn_exp2f(S[10]), __builtin_amdgcn_exp2f(S[11]));
      pb[2] = pk2(__builtin_amdgcn_exp2f(S[12]), __builtin_amdgcn_exp2f(S[13]));
      pb[3] = pk2(__builtin_amdgcn_exp2f(S[14]), __builtin_amdgcn_exp2f(S[15]));
      SB();
      o = MFMA32(vf[j & 1][1], __builtin_bit_cast(bf16x8, pb), o);
      SB();
    });
  } else {
    const int j0 = 8 - tbq;
    float m0 = NEG, m1 = NEG;
    for (int j = j0; j < 9; ++j) {
      f32x16 s = MFMA32(kfrag(j, 0), qa, zero); s = MFMA32(kfrag(j, 1), qb, s);
      if (j == 8) mask8(s);
      max8(s, 0, m0, m1); max8(s, 8, m0, m1);
    }
    float mx = fmaxf(m0, m1);
    { auto rr = __builtin_amdgcn_permlane32_swap(__float_as_uint(mx), __float_as_uint(mx), false, false);
      mx = fmaxf(__uint_as_float(rr[0]), __uint_as_float(rr[1])); }
    f32x16 negm;
#pragma unroll
    for (int r = 0; r < 16; ++r) negm[r] = -mx;
    for (int j = j0; j < 9; ++j) {
      f32x16 s = MFMA32(kfrag(j, 0), qa, negm); s = MFMA32(kfrag(j, 1), qb, s);
      if (j == 8) mask8(s);
      u32x4 pa, pb;
#pragma unroll
      for (int r = 0; r < 4; ++r) { pa[r] = pk2(__builtin_amdgcn_exp2f(s[2 * r]), __builtin_amdgcn_exp2f(s[2 * r + 1])); pb[r] = pk2(__builtin_amdgcn_exp2f(s[8 + 2 * r]), __builtin_amdgcn_exp2f(s[9 + 2 * r])); }
      o = MFMA32(vfrag(j, 0), __builtin_bit_cast(bf16x8, pa), o); o = MFMA32(vfrag(j, 1), __builtin_bit_cast(bf16x8, pb), o);
    }
  }
  STAMPW(6);
  __builtin_amdgcn_s_setprio(3);
  float l;
  { auto rr = __builtin_amdgcn_permlane32_swap(__float_as_uint(o[12]), __float_as_uint(o[12]), false, false); l = __uint_as_float(rr[1]); }
  const float scl = gq / l;
  u32x4 f0, f1;
  f0[0] = pk2(o[0] * scl, o[1] * scl); f0[1] = pk2(o[2] * scl, o[3] * scl); f0[2] = pk2(o[4] * scl, o[5] * scl); f0[3] = pk2(o[6] * scl, o[7] * scl);
  f1[0] = pk2(o[8] * scl, o[9] * scl); f1[1] = pk2(o[10] * scl, o[11] * scl); f1[2] = pk2(o[12] * scl, o[13] * scl); f1[3] = pk2(o[14] * scl, o[15] * scl);
  if (hh) { f1[2] = 0; f1[3] = 0; }
  {
    const auto rs = __builtin_amdgcn_make_buffer_rsrc((void*)ws, 0, (unsigned)WS_END, 0x00020000);
    const unsigned off = WS_O + (tbq * 16 + h) * 2048 + lane * 16;
    __builtin_amdgcn_raw_buffer_store_b128(f0, rs, off, 0, 16);
    __builtin_amdgcn_raw_buffer_store_b128(f1, rs, off + 1024, 0, 16);
  }
  publish(lds, CTL_OPUB, 4u, ws, FLAG_O + i * 16 + h, tag, lane);
  STAMPW(7);
  if (w4 == 0 && ig >= 0) {
    poll_flags(ws, FLAG_O + ig * 16 + (lane & 15), tag);
    if (lane == 0) lds_st(lds, CTL_ORDY, 1u);
    STAMPW(8);
  }
}

DI void c_gemm(LAS char* lds, const char* w2, const char* wo, char* ws, int rt, int ctile, int w, int lane, f32x16 (&acc)[2][2] STAMP_PARAM) {
  const unsigned ring0 = (unsigned)(size_t)lds + RING0 + w * RING_STRIDE;
  LAS char* ring = lds + RING0 + w * RING_STRIDE;
  const unsigned tag = lds_ld(lds, CTL_TAG);
  const int tb0 = 2 * rt;
  const char* hsrc = ws + WS_H + ((size_t)tb0 * 112 + 28 * w) * 1024;
  const char* ofr = ws + WS_O + ((size_t)tb0 * 16 + 4 * w) * 2048;
  const char* wsrc = w2 + (size_t)(448 * w) * 896 + ctile * 128;
  const char* osrc = wo + ctile * 128;
  const unsigned hv = lane * 16;
  const unsigned chs = 16 * ((lane & 7) ^ (4 * ((lane >> 4) & 1)));
  const unsigned wv = (lane >> 3) * 896 + chs;
  const int orow = ((lane >> 3) & 3) + 8 * (lane >> 5);
  u32x4 hr[C_D][2];
  auto issue1 = [&](auto S, auto Dn) {
    constexpr int s = S, d = Dn, slot = s % C_D;
    const unsigned lb = ring0 + slot * 2048;
    if constexpr (s < 28) {
      if constexpr (d == 0) glds16(wsrc + (size_t)(16 * s) * 896, wv, lb);
      if constexpr (d == 1) glds16(wsrc + (size_t)(16 * s + 8) * 896, wv, lb + 1024);
      if constexpr (d == 2) gld16_dev(hr[slot][0], hsrc + s * 1024, hv);
      if constexpr (d == 3) gld16_dev(hr[slot][1], hsrc + s * 1024 + 112 * 1024, hv);
    } else if constexpr (s < 36) {
      constexpr int o = s - 28;
      if constexpr (d < 2) {
        const int rb = 28 * (4 * w + (o >> 1)) + 16 * (o & 1);
        const int ra = min(rb + orow + 4 * d, 447);
        glds16(osrc, ra * 896 + chs, lb + d * 1024);
      }
      if constexpr (d == 2) gld16_dev(hr[slot][0], ofr + o * 1024, hv);
      if constexpr (d == 3) gld16_dev(hr[slot][1], ofr + o * 1024 + 16 * 2048, hv);
    }
  };
  const int hh = lane >> 5, blk = (lane >> 4) & 1, q4 = (lane & 15) >> 2, p4 = lane & 3;
  const unsigned tb_0 = (8 * hh + q4) * 128 + 16 * ((0 + 2 * blk + (p4 >> 1)) ^ (4 * ((q4 >> 1) & 1))) + 8 * (p4 & 1);
  const unsigned tb_1 = (8 * hh + q4) * 128 + 16 * ((4 + 2 * blk + (p4 >> 1)) ^ (4 * ((q4 >> 1) & 1))) + 8 * (p4 & 1);
  bf16x8 wf[2][2];
  auto ldw0 = [&](auto S) { constexpr int s = S; LAS const char* b = ring + (s % C_D) * 2048; wf[s & 1][0] = cat(ldtr(b + tb_0), ldtr(b + tb_0 + 512)); };
  auto ldw1 = [&](auto S) { constexpr int s = S; LAS const char* b = ring + (s % C_D) * 2048; wf[s & 1][1] = cat(ldtr(b + tb_1), ldtr(b + tb_1 + 512)); };
  static_for<0, C_D>([&](auto S) { issue1(S, IC<0>{}); issue1(S, IC<1>{}); });
  {
    const int l = lane & 3;
    poll_flags(ws, l < 2 ? (rt >> 1) * 14 + 6 + 2 * w + l : FLAG_G + rt, tag);
  }
  STAMPW(4);
  float g0, g1;
  asm volatile("global_load_dword %0, %2, %3 sc1\n\tglobal_load_dword %1, %2, %3 offset:128 sc1" : "=&v"(g0), "=&v"(g1) : "v"((lane & 31) * 4), "s"(ws + WS_G + tb0 * 128) : "memory");
  static_for<0, C_D>([&](auto S) { issue1(S, IC<2>{}); issue1(S, IC<3>{}); });
  ldw0(IC<0>{}); ldw1(IC<0>{});
  static_for<0, 36>([&](auto S) {
    constexpr int s = S, buf = s & 1, slot = s % C_D;
    wait_lgkm0();
    constexpr int allow = [] {
      if (s + 1 < C_D) { int n = 2 * (C_D - 1 - s); for (int t = C_D; t <= s + C_D - 1 && t < 36; ++t) n += 4; return n; }
      int n = 0; for (int t = s + 2; t <= s + C_D - 1 && t < 36; ++t) n += 4; return n; }();
    wait_vm<allow>();
    if constexpr (s == 0) STAMPW(5);
    asm volatile("" : "+v"(hr[slot][0]), "+v"(hr[slot][1]));
    const bf16x8 h0 = __builtin_bit_cast(bf16x8, hr[slot][0]), h1 = __builtin_bit_cast(bf16x8, hr[slot][1]);
    if constexpr (s == 28) {
      asm volatile("" : "+v"(g0), "+v"(g1));
      const float s0 = 1.f - g0, s1 = 1.f - g1;
#pragma unroll
      for (int ct = 0; ct < 2; ++ct) { acc[ct][0] *= s0; acc[ct][1] *= s1; }
    }
    if constexpr (s + C_D == 28) { lds_wait_ge(lds, CTL_ORDY, 1u); STAMPW(7); }
    SB();
    using SN = IC<s + C_D>;
    acc[0][0] = MFMA32(wf[buf][0], h0, acc[0][0]);
    if constexpr (s + 1 < 36) ldw0(IC<s + 1>{});
    issue1(SN{}, IC<0>{}); SB();
    acc[0][1] = MFMA32(wf[buf][0], h1, acc[0][1]);
    if constexpr (s + 1 < 36) ldw1(IC<s + 1>{});
    issue1(SN{}, IC<1>{}); SB();
    acc[1][0] = MFMA32(wf[buf][1], h0, acc[1][0]); SB();
    acc[1][1] = MFMA32(wf[buf][1], h1, acc[1][1]); SB();
    issue1(SN{}, IC<2>{});
    issue1(SN{}, IC<3>{}); SB();
    if constexpr (s == 13) STAMPW(6);
  });
  STAMPW(8);
}

__global__ __launch_bounds__(512, 2) void fused_block(const bf16* __restrict__ x, const bf16* __restrict__ wqkv, const bf16* __restrict__ wo, const bf16* __restrict__ w1,
                                                      const bf16* __restrict__ w2, const bf16* __restrict__ gw, char* __restrict__ ws, bf16* __restrict__ out) {
  __shared__ __attribute__((aligned(16))) char smem[LDS_BYTES];
  LAS char* lds = (LAS char*)smem;
  const int b = blockIdx.x, xcd = b & 7, idx = b >> 3, r = xcd >> 1, c2 = xcd & 1;
  const int tid = threadIdx.x, lane = tid & 63;
  const int w = __builtin_amdgcn_readfirstlane(tid >> 6);
  STAMP_DECL;
  STAMPW(0);
  const bool tile = idx < 28;
  const int i = 4 * r + idx / 7, cc = idx % 7;
  if (w == 0) {
    const unsigned e = __hip_atomic_load((GAS unsigned*)(ws + WS_EPOCH), __ATOMIC_RELAXED, __HIP_MEMORY_SCOPE_AGENT);
    if (lane == 0) {
      *(LAS u32x4*)(lds + 0) = (u32x4){e * 2654435761u + 0x7F4A7C15u, e, 0u, 0u};
      *(LAS u32x4*)(lds + 16) = (u32x4){0u, 0u, 0u, 0u};
    }
    wait_lgkm0();
  }
  const char* xg = (const char*)x + (size_t)i * 128 * 896;
  const char* wg = cc < 3 ? (const char*)wqkv + (2 * cc + c2) * 448 : (const char*)w1 + (4 * c2 + cc - 3) * 448;
  const int ldw2 = cc < 3 ? NQKV * 2 : DFF * 2;
  const int rt = 8 * r + 4 * c2 + idx / 7, ctile = idx % 7;
  if (w >= 4) {
    if (tile) a_loader(lds, xg, wg, ldw2, w - 4 STAMP_ARG); else raw_barrier();
    STAMPW(2);
    b_attention(lds, ws, 4 * r + (idx >> 3), 8 * c2 + (idx & 7), w - 4, lane, tile ? (rt >> 1) : -1 STAMP_ARG);
    if (b == 0 && w == 7) {
      const unsigned tag = lds_ld(lds, CTL_TAG);
#pragma unroll
      for (int k = 0; k < 4; ++k) poll_flags(ws, k * 64 + lane, tag);
      if (lane == 0) __hip_atomic_store((GAS unsigned*)(ws + WS_EPOCH), lds_ld(lds, CTL_EPOCH) + 1u, __ATOMIC_RELAXED, __HIP_MEMORY_SCOPE_AGENT);
    }
  } else if (tile) {
    if (cc == 2) a_compute<true>(lds, 2, i, c2, w, ws, xg, wg, ldw2, cc STAMP_ARG);
    else a_compute<false>(lds, cc < 3 ? cc : 3, i, cc < 3 ? c2 : 4 * c2 + cc - 3, w, ws, xg, wg, ldw2, cc STAMP_ARG);
    publish(lds, CTL_APUB, 4u, ws, i * 14 + (cc < 3 ? 2 * cc + c2 : 6 + 4 * c2 + cc - 3), lds_ld(lds, CTL_TAG), lane);
    STAMPW(3);
    f32x16 acc[2][2] = {};
    c_gemm(lds, (const char*)w2, (const char*)wo, ws, rt, ctile, w, lane, acc STAMP_ARG);
    wait_lgkm0();
    LAS char* red = lds + RING0 + w * RING_STRIDE;
    const int hh = lane >> 5;
#pragma unroll
    for (int ct = 0; ct < 2; ++ct)
#pragma unroll
      for (int tt = 0; tt < 2; ++tt)
#pragma unroll
        for (int rg = 0; rg < 4; ++rg) {
          const f32x4 v = {acc[ct][tt][4 * rg], acc[ct][tt][4 * rg + 1], acc[ct][tt][4 * rg + 2], acc[ct][tt][4 * rg + 3]};
          *(LAS f32x4*)(red + (32 * tt + (lane & 31)) * RED_STRIDE + (32 * ct + 8 * rg + 4 * hh) * 4) = v;
        }
  } else {
    raw_barrier();
    const int gi = (idx - 28) + 4 * xcd;
    gate_rows((const char*)x, (const char*)gw, (const char*)w2, (const char*)wo, ws, gi, idx - 28, w, lane);
    publish(lds, CTL_APUB, 4u, ws, FLAG_G + gi, lds_ld(lds, CTL_TAG), lane);
    warm_w2((const char*)w2, (const char*)wo, ws, idx - 28, tid);
  }
  STAMPW(9);
  if (!tile) { STAMP_FLUSH(); return; }
  __syncthreads();
  STAMPW(10);
  {
    const int tok = tid >> 3, cg = tid & 7;
    f32x4 a = {0, 0, 0, 0}, c = {0, 0, 0, 0};
#pragma unroll
    for (int ww = 0; ww < 4; ++ww) {
      a += *(LAS const f32x4*)(lds + RING0 + ww * RING_STRIDE + tok * RED_STRIDE + cg * 32);
      c += *(LAS const f32x4*)(lds + RING0 + ww * RING_STRIDE + tok * RED_STRIDE + cg * 32 + 16);
    }
    const u32x4 v = {pk2(a[0], a[1]), pk2(a[2], a[3]), pk2(c[0], c[1]), pk2(c[2], c[3])};
    *(u32x4*)((char*)out + (size_t)(rt * 64 + tok) * 896 + ctile * 128 + cg * 16) = v;
  }
  wait_vm<0>(); STAMPW(11);
  STAMP_FLUSH();
}

extern "C" void kernel_launch(void* const* d_in, const int* in_sizes, int n_in, void* d_out, int out_size, void* d_ws, size_t ws_size, hipStream_t stream) {
  fused_block<<<256, 512, 0, stream>>>((const bf16*)d_in[0], (const bf16*)d_in[1], (const bf16*)d_in[2], (const bf16*)d_in[4], (const bf16*)d_in[5], (const bf16*)d_in[6],
                                       (char*)d_ws, (bf16*)d_out);
}
```

```cpp
#define STAMP
#define LEAKSKEW 2
#include <hip/hip_runtime.h>
#include <hip/hip_bf16.h>
#include <utility>
#include <type_traits>

typedef __hip_bfloat16 bf16;
typedef short bf16x8 __attribute__((ext_vector_type(8)));
typedef short s16x4 __attribute__((ext_vector_type(4)));
typedef short s16x2 __attribute__((ext_vector_type(2)));
typedef float f32x4 __attribute__((ext_vector_type(4)));
typedef float f32x2 __attribute__((ext_vector_type(2)));
typedef float f32x16 __attribute__((ext_vector_type(16)));
typedef unsigned u32x4 __attribute__((ext_vector_type(4)));
typedef __bf16 bf16x2_t __attribute__((ext_vector_type(2)));
typedef short v4i16_t __attribute__((ext_vector_type(4)));
#define LAS __attribute__((address_space(3)))
#define GAS __attribute__((address_space(1)))
#define DI __device__ __forceinline__
#define SB() __builtin_amdgcn_sched_barrier(0)
#define MFMA16(a, b, c) __builtin_amdgcn_mfma_f32_16x16x32_bf16(a, b, c, 0, 0, 0)
#define MFMA32(a, b, c) __builtin_amdgcn_mfma_f32_32x32x16_bf16(a, b, c, 0, 0, 0)

constexpr int NQKV = 1344, DFF = 1792;
constexpr size_t WS_FLAG  = 0;
constexpr size_t WS_EPOCH = 65536;
constexpr size_t WS_G     = 69632;
constexpr size_t WS_Q     = 131072;
constexpr size_t FRAG_TB  = 28 * 1024;
constexpr size_t WS_K     = WS_Q + 64 * FRAG_TB;
constexpr size_t WS_V     = WS_K + 64 * FRAG_TB;
constexpr size_t WS_H     = WS_V + 64 * FRAG_TB + 4096;
constexpr size_t WS_O     = WS_H + 64 * 112 * 1024;
constexpr size_t WS_END   = WS_O + 64 * 16 * 2048;
constexpr int FLAG_G = 224, FLAG_O = 256;
constexpr int CTL_TAG = 0, CTL_EPOCH = 4, CTL_APUB = 8, CTL_KV = 12, CTL_OPUB = 16, CTL_ORDY = 20, CTL_QRDY = 24;
constexpr int A_BASE = 64, A_XB = 16384, A_WB = 28672, A_SLOT = A_XB + A_WB;
constexpr int KV_K = A_BASE + A_SLOT, KV_V = KV_K + 24576;
constexpr int RING0 = KV_V + 24576, RING_STRIDE = 17392, RED_STRIDE = 272;
constexpr int LDS_BYTES = RING0 + 4 * RING_STRIDE;
constexpr int C_D = 8;
static_assert(LDS_BYTES == 163840 && C_D * 2048 <= RING_STRIDE, "lds");

#define STAMP_KERNELS 1
constexpr size_t STAMP_OFF = 32u << 20;
struct Stamps { unsigned long long t[12]; unsigned long long c0, c1; };
#define STAMP_DECL Stamps stv; for (int q_ = 0; q_ < 12; ++q_) stv.t[q_] = 0; stv.c0 = __builtin_amdgcn_s_memtime(); stv.c1 = 0
#define STAMPW(j) do { stv.t[j] = __builtin_amdgcn_s_memrealtime(); if ((j) == 9) stv.c1 = __builtin_amdgcn_s_memtime(); } while (0)
#define STAMP_FLUSH() do { if ((threadIdx.x & 63) == 0) { unsigned long long* p_ = (unsigned long long*)(ws + STAMP_OFF) + (blockIdx.x * 8 + (threadIdx.x >> 6)) * 16; \
    for (int q_ = 0; q_ < 12; ++q_) p_[q_] = stv.t[q_]; \
    unsigned long long* c_ = (unsigned long long*)(ws + STAMP_OFF + (4u << 20)) + (blockIdx.x * 8 + (threadIdx.x >> 6)) * 2; c_[0] = stv.c0; c_[1] = stv.c1; } } while (0)
#undef STAMP_FLUSH
#define STAMP_FLUSH() do { if (blockIdx.x == 100 && (threadIdx.x >> 6) == 0) { \
    unsigned long long mn_ = ~0ull, mx_ = 0; \
    for (int q_ = 0; q_ < 4; ++q_) { unsigned long long v_ = __hip_atomic_load((GAS unsigned long long*)(ws + (40u << 20)) + q_ * 64 + (threadIdx.x & 63), __ATOMIC_RELAXED, __HIP_MEMORY_SCOPE_AGENT); mn_ = v_ < mn_ ? v_ : mn_; mx_ = v_ > mx_ ? v_ : mx_; } \
    for (int o_ = 32; o_; o_ >>= 1) { unsigned long long a_ = __shfl_xor(mn_, o_), b_ = __shfl_xor(mx_, o_); mn_ = a_ < mn_ ? a_ : mn_; mx_ = b_ > mx_ ? b_ : mx_; } \
    const unsigned long long d_ = LEAKSKEW == 1 ? mx_ - mn_ : (LEAKSKEW == 2 ? stv.t[0] - mn_ : __builtin_amdgcn_s_memrealtime() - mn_); \
    const unsigned long long e_ = __builtin_amdgcn_s_memrealtime() + 20ull * (d_ < 5000 ? d_ : 5000); \
    for (int q_ = 0; q_ < 2000000 && __builtin_amdgcn_s_memrealtime() < e_; ++q_) __builtin_amdgcn_s_sleep(8); } } while (0)
#define STAMP_PARAM , Stamps& stv
#define STAMP_ARG , stv

template <int I, int N, class F> DI void static_for(F&& f) {
  if constexpr (I < N) { f(std::integral_constant<int, I>{}); static_for<I + 1, N>(f); }
}
template <int V> using IC = std::integral_constant<int, V>;
template <int N> DI void wait_vm() { asm volatile("s_waitcnt vmcnt(%0)" ::"n"(N) : "memory"); }
DI void wait_lgkm0() { asm volatile("s_waitcnt lgkmcnt(0)" ::: "memory"); }
DI void raw_barrier() { asm volatile("s_barrier" ::: "memory"); }
DI void glds16(const void* sbase, unsigned voff, unsigned lds_addr) {
  unsigned keep;
  asm volatile("s_mov_b32 %0, m0\n\ts_mov_b32 m0, %3\n\ts_nop 0\n\tglobal_load_lds_dwordx4 %1, %2\n\ts_mov_b32 m0, %0"
               : "=&s"(keep) : "v"(voff), "s"(sbase), "s"(lds_addr) : "memory");
}
DI void glds16_dev(const void* sbase, unsigned voff, unsigned lds_addr) {
  unsigned keep;
  asm volatile("s_mov_b32 %0, m0\n\ts_mov_b32 m0, %3\n\ts_nop 0\n\tglobal_load_lds_dwordx4 %1, %2 sc1\n\ts_mov_b32 m0, %0"
               : "=&s"(keep) : "v"(voff), "s"(sbase), "s"(lds_addr) : "memory");
}
DI void gld16_dev(u32x4& d, const void* sbase, unsigned voff) {
  asm volatile("global_load_dwordx4 %0, %1, %2 sc1" : "=v"(d) : "v"(voff), "s"(sbase) : "memory");
}
DI unsigned pk2(float a, float b) { return __builtin_bit_cast(unsigned, __builtin_convertvector((f32x2){a, b}, bf16x2_t)); }
DI unsigned relu2(unsigned x) { return __builtin_bit_cast(unsigned, __builtin_elementwise_max(__builtin_bit_cast(s16x2, x), (s16x2){0, 0})); }
DI s16x4 ldtr(LAS const char* p) { return __builtin_bit_cast(s16x4, __builtin_amdgcn_ds_read_tr16_b64_v4i16((LAS v4i16_t*)p)); }
DI bf16x8 cat(s16x4 lo, s16x4 hi) { return __builtin_shufflevector(lo, hi, 0, 1, 2, 3, 4, 5, 6, 7); }
DI unsigned lds_ld(LAS char* lds, int off) { return __hip_atomic_load((LAS unsigned*)(lds + off), __ATOMIC_RELAXED, __HIP_MEMORY_SCOPE_WORKGROUP); }
DI void lds_st(LAS char* lds, int off, unsigned v) { __hip_atomic_store((LAS unsigned*)(lds + off), v, __ATOMIC_RELAXED, __HIP_MEMORY_SCOPE_WORKGROUP); }
DI void lds_wait_ge(LAS char* lds, int off, unsigned v) {
  for (unsigned sp = 0; sp < (1u << 20) && lds_ld(lds, off) < v; ++sp) __builtin_amdgcn_s_sleep(1);
  asm volatile("" ::: "memory");
}
DI GAS unsigned* flag_ptr(char* ws, int idx) { return (GAS unsigned*)(ws + WS_FLAG) + idx * 32; }
DI void poll_flags(char* ws, int idx, unsigned tag) {
  GAS unsigned* fl = flag_ptr(ws, idx);
  for (unsigned sp = 0; sp < (1u << 18); ++sp) {
    const unsigned v = __hip_atomic_load(fl, __ATOMIC_RELAXED, __HIP_MEMORY_SCOPE_AGENT);
    if (__all(v == tag)) break;
    __builtin_amdgcn_s_sleep(2);
  }
  asm volatile("" ::: "memory");
}
DI void publish(LAS char* lds, int cnt_off, unsigned n, char* ws, int flag, unsigned tag, int lane) {
  wait_vm<0>();
  unsigned old = 0;
  if (lane == 0) old = __hip_atomic_fetch_add((LAS unsigned*)(lds + cnt_off), 1u, __ATOMIC_RELAXED, __HIP_MEMORY_SCOPE_WORKGROUP);
  old = __builtin_amdgcn_readfirstlane(old);
  if (old == n - 1 && lane == 0) __hip_atomic_store(flag_ptr(ws, flag), tag, __ATOMIC_RELAXED, __HIP_MEMORY_SCOPE_AGENT);
}

DI void a_loader(LAS char* lds, const char* xg, const char* wg, int ldw2, int w STAMP_PARAM) {
  const int lane = threadIdx.x & 63;
  const unsigned lds0 = (unsigned)(size_t)lds + A_BASE;
  unsigned xo[4], wo_[7];
#pragma unroll
  for (int tt = 0; tt < 4; ++tt) xo[tt] = (32 * w + 8 * tt + (lane >> 3)) * 896 + 16 * ((lane & 7) ^ ((lane >> 4) | (4 * (tt & 1))));
#pragma unroll
  for (int tt = 0; tt < 7; ++tt) { const int q = (7 * w + tt) * 64 + lane, rho = q / 28, cp = q % 28; wo_[tt] = rho * ldw2 + 16 * (cp ^ (2 * ((rho >> 3) & 1))); }
  auto stage = [&](int kc, int slot) {
    const char* xs = xg + kc * 128; const char* wsrc = wg + (size_t)kc * 64 * ldw2;
    const unsigned lb = lds0 + slot * A_SLOT;
#pragma unroll
    for (int tt = 0; tt < 4; ++tt) glds16(xs, xo[tt], lb + (4 * w + tt) * 1024);
#pragma unroll
    for (int tt = 0; tt < 7; ++tt) glds16(wsrc, wo_[tt], lb + A_XB + (7 * w + tt) * 1024);
  };
  stage(0, 0); stage(1, 1);
  wait_vm<11>(); raw_barrier();
  STAMPW(1);
  stage(2, 2);
  static_for<0, 6>([&](auto C) {
    constexpr int c = C;
    if constexpr (c + 2 <= 6) wait_vm<11>(); else wait_vm<0>();
    raw_barrier();
    if constexpr (c + 3 <= 6) stage(c + 3, c % 3);
  });
}

template <bool VT>
DI void a_compute(LAS char* lds0, int kind  , int i, int cgl, int w, char* ws, const char* xg, const char* wg, int ldw2, int cc STAMP_PARAM) {
  LAS char* lds = lds0 + A_BASE;
  const int lane = threadIdx.x & 63;
  const int wm = w >> 1, wn = w & 1;
  const int m = lane & 15, g = lane >> 4, q4 = (lane & 15) >> 2, p4 = lane & 3;
  const unsigned xb0 = (64 * wm + m) * 128 + 16 * ((g) ^ (m >> 1));
  const unsigned xb1 = (64 * wm + m) * 128 + 16 * ((4 + g) ^ (m >> 1));
  const unsigned wbb = A_XB + (8 * g + q4) * 448 + 8 * p4 + 7 * wn * 32;
  const unsigned wE = wbb + ((wn & 1) ? -32 * (g & 1) : 32 * (g & 1));
  const unsigned wO = wbb + ((wn & 1) ? 32 * (g & 1) : -32 * (g & 1));
  f32x4 acc[4][7];
#pragma unroll
  for (int a = 0; a < 4; ++a)
#pragma unroll
    for (int t = 0; t < 7; ++t) acc[a][t] = (f32x4){0.f, 0.f, 0.f, 0.f};
  bf16x8 xf[2][4]; s16x4 wl[2][7], wh[2][7];
  auto rd = [&](auto BUF, auto SLOT, auto KK, auto R) {
    constexpr int buf = BUF, slot = SLOT, kk = KK, r = R;
    LAS const char* base = lds + slot * A_SLOT;
    if constexpr (r < 4) xf[buf][r] = *(LAS const bf16x8*)(base + (kk ? xb1 : xb0) + r * 2048);
    else {
      constexpr int tl = (r - 4) >> 1, hi = (r - 4) & 1;
      const unsigned b = ((tl & 1) ? wO : wE) + tl * 32 + kk * 32 * 448 + hi * 4 * 448;
      if constexpr (hi) wh[buf][tl] = ldtr(base + b); else wl[buf][tl] = ldtr(base + b);
    }
  };
  auto mm = [&](auto BUF, auto J) {
    constexpr int buf = BUF, j = J, tl = j >> 2, a = j & 3;
    const bf16x8 wf = cat(wl[buf][tl], wh[buf][tl]);
    acc[a][tl] = VT ? MFMA16(xf[buf][a], wf, acc[a][tl]) : MFMA16(wf, xf[buf][a], acc[a][tl]);
  };
  raw_barrier();
  STAMPW(1);
  unsigned t0 = 0, t1 = 0;
  {
    const int il = i & 3, e = w * 64 + lane;
    const char* wlp = wg - ((size_t)wg & 64);
    const unsigned o0 = (192 + 4 * (e >> 2) + il) * ldw2 + (e & 3) * 128;
    const int xe = min(cc * 74 + (e % 74), 511);
    const unsigned o1 = (xe >> 2) * 896 + (3 + (xe & 3)) * 128;
    asm volatile("global_load_dword %0, %1, %2" : "=&v"(t0) : "v"(o0), "s"(wlp) : "memory");
    if (w < 2) asm volatile("global_load_dword %0, %1, %2" : "=&v"(t1) : "v"(o1), "s"(xg) : "memory");
  }
  static_for<0, 18>([&](auto R) { rd(IC<0>{}, IC<0>{}, IC<0>{}, R); });
  SB();
  static_for<0, 7>([&](auto C) {
    constexpr int c = C, slot = c % 3, nslot = (c + 1) % 3;
    static_for<0, 28>([&](auto J) {
      mm(IC<0>{}, J);
      if constexpr (J < 18) rd(IC<1>{}, IC<slot>{}, IC<1>{}, J);
      SB();
    });
    if constexpr (c < 6) {
      wait_lgkm0(); raw_barrier(); SB();
      static_for<0, 28>([&](auto J) {
        mm(IC<1>{}, J);
        if constexpr (J < 18) rd(IC<0>{}, IC<nslot>{}, IC<0>{}, J);
        SB();
      });
    } else {
      static_for<0, 28>([&](auto J) { mm(IC<1>{}, J); });
    }
  });
  STAMPW(2);
  wait_vm<0>(); asm volatile("" :: "v"(t0), "v"(t1));
  const auto rs = __builtin_amdgcn_make_buffer_rsrc((void*)ws, 0, (unsigned)WS_END, 0x00020000);
  const int tb0 = 4 * i + 2 * wm;
  if constexpr (!VT) {
    unsigned dst; int ntile;
    if (kind == 0) { dst = WS_Q; ntile = 28; }
    else if (kind == 1) { dst = WS_K; ntile = 28; }
    else { dst = WS_H; ntile = 112; }
    dst += (tb0 * ntile + cgl * 14 + 7 * wn) * 1024 + lane * 16;
    if (kind == 0) {
      const float sc = 0.18898223650461362f * 1.4426950408889634f;
#pragma unroll
      for (int tl = 0; tl < 7; ++tl)
#pragma unroll
        for (int a = 0; a < 4; ++a) acc[a][tl] *= sc;
    }
    const bool relu = kind == 3;
#pragma unroll
    for (int tl = 0; tl < 7; ++tl)
#pragma unroll
      for (int pr = 0; pr < 2; ++pr) {
        const f32x4 X = acc[2 * pr][tl], Y = acc[2 * pr + 1][tl];
        unsigned x0 = pk2(X[0], X[1]), x1 = pk2(X[2], X[3]), y0 = pk2(Y[0], Y[1]), y1 = pk2(Y[2], Y[3]);
        if (relu) { x0 = relu2(x0); x1 = relu2(x1); y0 = relu2(y0); y1 = relu2(y1); }
        auto r0 = __builtin_amdgcn_permlane16_swap(x0, y0, false, false);
        auto r1 = __builtin_amdgcn_permlane16_swap(x1, y1, false, false);
        __builtin_amdgcn_raw_buffer_store_b128((u32x4){r0[0], r1[0], r0[1], r1[1]}, rs, dst + (pr * ntile + tl) * 1024, 0, 16);
      }
  } else {
    const int row = lane >> 4;
    const unsigned dst = WS_V + (((2 * tb0 + (row & 1)) * 2 + (row >> 1)) * 448 + cgl * 224 + 112 * wn + (lane & 15)) * 16;
#pragma unroll
    for (int tl = 0; tl < 7; ++tl)
#pragma unroll
      for (int pr = 0; pr < 2; ++pr) {
        const f32x4 X = acc[2 * pr][tl], Y = acc[2 * pr + 1][tl];
        unsigned x0 = pk2(X[0], X[1]), x1 = pk2(X[2], X[3]), y0 = pk2(Y[0], Y[1]), y1 = pk2(Y[2], Y[3]);
        auto r0 = __builtin_amdgcn_permlane16_swap(x0, y0, false, false);
        auto r1 = __builtin_amdgcn_permlane16_swap(x1, y1, false, false);
        __builtin_amdgcn_raw_buffer_store_b128((u32x4){r0[0], r1[0], r0[1], r1[1]}, rs, dst + (pr * 4 * 448 + 16 * tl) * 16, 0, 16);
      }
  }
}

DI void gate_rows(const char* x, const char* gw, const char* w2, const char* wo, char* ws, int gi, int part4, int w, int lane) {
  const int row = gi * 64 + w * 16 + (lane >> 2), part = lane & 3;
  float s = 0.f;
#pragma unroll
  for (int k = 0; k < 14; ++k) {
    const u32x4 xv = *(const u32x4*)(x + (size_t)row * 896 + k * 64 + part * 16);
    const u32x4 gv = *(const u32x4*)(gw + k * 64 + part * 16);
#pragma unroll
    for (int e = 0; e < 4; ++e) {
      s += __uint_as_float(xv[e] << 16) * __uint_as_float(gv[e] << 16);
      s += __uint_as_float(xv[e] & 0xffff0000u) * __uint_as_float(gv[e] & 0xffff0000u);
    }
  }
  s += __shfl_xor(s, 1); s += __shfl_xor(s, 2);
  if (part == 0) __hip_atomic_store((GAS float*)(ws + WS_G) + row, 1.f / (1.f + __expf(-s)), __ATOMIC_RELAXED, __HIP_MEMORY_SCOPE_AGENT);
}
DI void warm_w2(const char* w2, const char* wo, char* ws, int part4, int t256) {
  unsigned acc = 0;
#pragma unroll
  for (int k = 0; k < 16; ++k) {
    const int e = k * 256 + t256, rr = part4 * 560 + e / 7, ln = e % 7;
    if (e < 560 * 7) {
      const char* src = rr < 1792 ? w2 + (size_t)rr * 896 : wo + (size_t)(rr - 1792) * 896;
      acc ^= *(const unsigned*)(src + ln * 128);
    }
  }
  asm volatile("" :: "v"(acc));
}

DI void b_attention(LAS char* lds, char* ws, int i, int h, int w4, int lane, int ig   STAMP_PARAM) {
  const unsigned lds0 = (unsigned)(size_t)lds;
  const unsigned tag = lds_ld(lds, CTL_TAG);
  const int u0 = (h & 1) ? (28 * h - 4) / 8 : (28 * h) / 8;
  const int hh = lane >> 5, ql = lane & 31;
  __builtin_amdgcn_s_setprio(3);
  if (w4 == 0) {
    const int hg = h >> 3, l = lane & 15;
    int f;
    if (l < 3) f = max(i - l, 0) * 14 + 2 + hg;
    else if (l < 6) f = max(i - (l - 3), 0) * 14 + 4 + hg;
    else if (l < 8) f = FLAG_G + 2 * i + (l - 6);
    else f = i * 14 + hg;
    poll_flags(ws, f, tag);
    if (lane == 0) lds_st(lds, CTL_QRDY, 1u);
  } else {
    lds_wait_ge(lds, CTL_QRDY, 1u);
  }
  STAMPW(3);
  {
    const unsigned kvo = lane * 16, vvo = (hh * 448 + 28 * h + ql) * 16;
#pragma unroll
    for (int bq = 0; bq < 3; ++bq) {
      const int bb = 3 * w4 + bq; int tb = 4 * i - 8 + bb; tb = tb < 0 ? 0 : tb;
#pragma unroll
      for (int s = 0; s < 2; ++s) {
        glds16_dev(ws + WS_K + (size_t)tb * FRAG_TB + (u0 + 2 * s) * 512, kvo, lds0 + KV_K + bb * 2048 + s * 1024);
        glds16_dev(ws + WS_V + (size_t)(2 * tb + s) * 2 * 448 * 16, vvo, lds0 + KV_V + bb * 2048 + s * 1024);
      }
    }
  }
  const int tbq = 4 * i + w4;
  u32x4 q0, q1; float gq;
  gld16_dev(q0, ws + WS_Q + (size_t)tbq * FRAG_TB + u0 * 512, lane * 16);
  gld16_dev(q1, ws + WS_Q + (size_t)tbq * FRAG_TB + (u0 + 2) * 512, lane * 16);
  asm volatile("global_load_dword %0, %1, %2 sc1" : "=v"(gq) : "v"(ql * 4), "s"(ws + WS_G + tbq * 128) : "memory");
  wait_vm<0>();
  asm volatile("" : "+v"(q0), "+v"(q1), "+v"(gq));
  if (h & 1) { if (hh == 0) { q0[0] = 0; q0[1] = 0; } } else { if (hh == 1) { q1[2] = 0; q1[3] = 0; } }
  if (ql >= 28) {
    const u32x4 ones = {0x3f803f80u, 0x3f803f80u, 0x3f803f80u, 0x3f803f80u};
#pragma unroll
    for (int bq = 0; bq < 3; ++bq)
#pragma unroll
      for (int s = 0; s < 2; ++s) *(LAS u32x4*)(lds + KV_V + (3 * w4 + bq) * 2048 + s * 1024 + lane * 16) = ones;
  }
  wait_lgkm0();
  if (lane == 0) __hip_atomic_fetch_add((LAS unsigned*)(lds + CTL_KV), 1u, __ATOMIC_RELAXED, __HIP_MEMORY_SCOPE_WORKGROUP);
  lds_wait_ge(lds, CTL_KV, 4u);
  __builtin_amdgcn_s_setprio(0);
  STAMPW(4);
  const bf16x8 qa = __builtin_bit_cast(bf16x8, q0), qb = __builtin_bit_cast(bf16x8, q1);
  const int sw = (ql & 0x13) | ((ql & 4) << 1) | ((ql & 8) >> 1);
  LAS const char* kp = lds + KV_K + w4 * 2048 + hh * 512 + sw * 16;
  LAS const char* vp = lds + KV_V + w4 * 2048 + lane * 16;
  const f32x16 zero = {};
  const float NEG = -1e30f;
  auto mask0 = [&](f32x16& s) {
#pragma unroll
    for (int r = 0; r < 16; ++r) { const int kap = (r & 7) + 16 * (r >> 3) + 8 * hh; s[r] = (kap > ql) ? s[r] : NEG; } };
  auto mask8 = [&](f32x16& s) {
#pragma unroll
    for (int r = 0; r < 16; ++r) { const int kap = (r & 7) + 16 * (r >> 3) + 8 * hh; s[r] = (kap <= ql) ? s[r] : NEG; } };
  auto kfrag = [&](int j, int s) { return *(LAS const bf16x8*)(kp + j * 2048 + s * 1024); };
  auto vfrag = [&](int j, int s) { return *(LAS const bf16x8*)(vp + j * 2048 + s * 1024); };
  auto max8 = [&](const f32x16& s, int b, float& m0, float& m1) {
    m0 = fmaxf(fmaxf(m0, s[b]), s[b + 1]); m1 = fmaxf(fmaxf(m1, s[b + 2]), s[b + 3]);
    m0 = fmaxf(fmaxf(m0, s[b + 4]), s[b + 5]); m1 = fmaxf(fmaxf(m1, s[b + 6]), s[b + 7]); };
  f32x16 o = zero;
  if (tbq >= 8) {
    float m0 = NEG, m1 = NEG;
    bf16x8 kf[3][2]; f32x16 sv[2];
    kf[0][0] = kfrag(0, 0); kf[0][1] = kfrag(0, 1); kf[1][0] = kfrag(1, 0); kf[1][1] = kfrag(1, 1);
    SB();
    static_for<0, 10>([&](auto J) {
      constexpr int j = J;
      if constexpr (j < 9) sv[j & 1] = MFMA32(kf[j % 3][0], qa, zero);
      if constexpr (j + 2 < 11) { kf[(j + 2) % 3][0] = kfrag((j + 2) % 9, 0); kf[(j + 2) % 3][1] = kfrag((j + 2) % 9, 1); }
      SB();
      if constexpr (j - 1 == 0) mask0(sv[0]);
      if constexpr (j - 1 == 8) mask8(sv[0]);
      if constexpr (j >= 1) max8(sv[(j - 1) & 1], 0, m0, m1);
      SB();
      if constexpr (j < 9) sv[j & 1] = MFMA32(kf[j % 3][1], qb, sv[j & 1]);
      SB();
      if constexpr (j >= 1) max8(sv[(j - 1) & 1], 8, m0, m1);
      SB();
    });
    float mx = fmaxf(m0, m1);
    { auto rr = __builtin_amdgcn_permlane32_swap(__float_as_uint(mx), __float_as_uint(mx), false, false);
      mx = fmaxf(__uint_as_float(rr[0]), __uint_as_float(rr[1])); }
    f32x16 negm;
#pragma unroll
    for (int r = 0; r < 16; ++r) negm[r] = -mx;
    STAMPW(5);
    bf16x8 vf[2][2];
    vf[0][0] = vfrag(0, 0); vf[0][1] = vfrag(0, 1);
    kf[2][0] = kfrag(2, 0); kf[2][1] = kfrag(2, 1);
    sv[0] = MFMA32(kf[0][0], qa, negm); sv[0] = MFMA32(kf[0][1], qb, sv[0]);
    SB();
    static_for<0, 9>([&](auto J) {
      constexpr int j = J;
      f32x16& S = sv[j & 1];
      u32x4 pa, pb;
      if constexpr (j + 1 < 9) sv[(j + 1) & 1] = MFMA32(kf[(j + 1) % 3][0], qa, negm);
      if constexpr (j + 1 < 9) { vf[(j + 1) & 1][0] = vfrag(j + 1, 0); vf[(j + 1) & 1][1] = vfrag(j + 1, 1); }
      SB();
      if constexpr (j == 0) mask0(sv[0]);
      if constexpr (j == 8) mask8(sv[0]);
      pa[0] = pk2(__builtin_amdgcn_exp2f(S[0]), __builtin_amdgcn_exp2f(S[1]));
      pa[1] = pk2(__builtin_amdgcn_exp2f(S[2]), __builtin_amdgcn_exp2f(S[3]));
      SB();
      if constexpr (j + 1 < 9) sv[(j + 1) & 1] = MFMA32(kf[(j + 1) % 3][1], qb, sv[(j + 1) & 1]);
      if constexpr (j + 3 < 9) { kf[j % 3][0] = kfrag(j + 3, 0); kf[j % 3][1] = kfrag(j + 3, 1); }
      SB();
      pa[2] = pk2(__builtin_amdgcn_exp2f(S[4]), __builtin_amdgcn_exp2f(S[5]));
      pa[3] = pk2(__builtin_amdgcn_exp2f(S[6]), __builtin_amdgcn_exp2f(S[7]));
      pb[0] = pk2(__builtin_amdgcn_exp2f(S[8]), __builtin_amdgcn_exp2f(S[9]));
      SB();
      o = MFMA32(vf[j & 1][0], __builtin_bit_cast(bf16x8, pa), o);
      SB();
      pb[1] = pk2(__builtin_amdgcn_exp2f(S[10]), __builtin_amdgcn_exp2f(S[11]));
      pb[2] = pk2(__builtin_amdgcn_exp2f(S[12]), __builtin_amdgcn_exp2f(S[13]));
      pb[3] = pk2(__builtin_amdgcn_exp2f(S[14]), __builtin_amdgcn_exp2f(S[15]));
      SB();
      o = MFMA32(vf[j & 1][1], __builtin_bit_cast(bf16x8, pb), o);
      SB();
    });
  } else {
    const int j0 = 8 - tbq;
    float m0 = NEG, m1 = NEG;
    for (int j = j0; j < 9; ++j) {
      f32x16 s = MFMA32(kfrag(j, 0), qa, zero); s = MFMA32(kfrag(j, 1), qb, s);
      if (j == 8) mask8(s);
      max8(s, 0, m0, m1); max8(s, 8, m0, m1);
    }
    float mx = fmaxf(m0, m1);
    { auto rr = __builtin_amdgcn_permlane32_swap(__float_as_uint(mx), __float_as_uint(mx), false, false);
      mx = fmaxf(__uint_as_float(rr[0]), __uint_as_float(rr[1])); }
    f32x16 negm;
#pragma unroll
    for (int r = 0; r < 16; ++r) negm[r] = -mx;
    for (int j = j0; j < 9; ++j) {
      f32x16 s = MFMA32(kfrag(j, 0), qa, negm); s = MFMA32(kfrag(j, 1), qb, s);
      if (j == 8) mask8(s);
      u32x4 pa, pb;
#pragma unroll
      for (int r = 0; r < 4; ++r) { pa[r] = pk2(__builtin_amdgcn_exp2f(s[2 * r]), __builtin_amdgcn_exp2f(s[2 * r + 1])); pb[r] = pk2(__builtin_amdgcn_exp2f(s[8 + 2 * r]), __builtin_amdgcn_exp2f(s[9 + 2 * r])); }
      o = MFMA32(vfrag(j, 0), __builtin_bit_cast(bf16x8, pa), o); o = MFMA32(vfrag(j, 1), __builtin_bit_cast(bf16x8, pb), o);
    }
  }
  STAMPW(6);
  __builtin_amdgcn_s_setprio(3);
  float l;
  { auto rr = __builtin_amdgcn_permlane32_swap(__float_as_uint(o[12]), __float_as_uint(o[12]), false, false); l = __uint_as_float(rr[1]); }
  const float scl = gq / l;
  u32x4 f0, f1;
  f0[0] = pk2(o[0] * scl, o[1] * scl); f0[1] = pk2(o[2] * scl, o[3] * scl); f0[2] = pk2(o[4] * scl, o[5] * scl); f0[3] = pk2(o[6] * scl, o[7] * scl);
  f1[0] = pk2(o[8] * scl, o[9] * scl); f1[1] = pk2(o[10] * scl, o[11] * scl); f1[2] = pk2(o[12] * scl, o[13] * scl); f1[3] = pk2(o[14] * scl, o[15] * scl);
  if (hh) { f1[2] = 0; f1[3] = 0; }
  {
    const auto rs = __builtin_amdgcn_make_buffer_rsrc((void*)ws, 0, (unsigned)WS_END, 0x00020000);
    const unsigned off = WS_O + (tbq * 16 + h) * 2048 + lane * 16;
    __builtin_amdgcn_raw_buffer_store_b128(f0, rs, off, 0, 16);
    __builtin_amdgcn_raw_buffer_store_b128(f1, rs, off + 1024, 0, 16);
  }
  publish(lds, CTL_OPUB, 4u, ws, FLAG_O + i * 16 + h, tag, lane);
  STAMPW(7);
  if (w4 == 0 && ig >= 0) {
    poll_flags(ws, FLAG_O + ig * 16 + (lane & 15), tag);
    if (lane == 0) lds_st(lds, CTL_ORDY, 1u);
    STAMPW(8);
  }
}

DI void c_gemm(LAS char* lds, const char* w2, const char* wo, char* ws, int rt, int ctile, int w, int lane, f32x16 (&acc)[2][2] STAMP_PARAM) {
  const unsigned ring0 = (unsigned)(size_t)lds + RING0 + w * RING_STRIDE;
  LAS char* ring = lds + RING0 + w * RING_STRIDE;
  const unsigned tag = lds_ld(lds, CTL_TAG);
  const int tb0 = 2 * rt;
  const char* hsrc = ws + WS_H + ((size_t)tb0 * 112 + 28 * w) * 1024;
  const char* ofr = ws + WS_O + ((size_t)tb0 * 16 + 4 * w) * 2048;
  const char* wsrc = w2 + (size_t)(448 * w) * 896 + ctile * 128;
  const char* osrc = wo + ctile * 128;
  const unsigned hv = lane * 16;
  const unsigned chs = 16 * ((lane & 7) ^ (4 * ((lane >> 4) & 1)));
  const unsigned wv = (lane >> 3) * 896 + chs;
  const int orow = ((lane >> 3) & 3) + 8 * (lane >> 5);
  u32x4 hr[C_D][2];
  auto issue1 = [&](auto S, auto Dn) {
    constexpr int s = S, d = Dn, slot = s % C_D;
    const unsigned lb = ring0 + slot * 2048;
    if constexpr (s < 28) {
      if constexpr (d == 0) glds16(wsrc + (size_t)(16 * s) * 896, wv, lb);
      if constexpr (d == 1) glds16(wsrc + (size_t)(16 * s + 8) * 896, wv, lb + 1024);
      if constexpr (d == 2) gld16_dev(hr[slot][0], hsrc + s * 1024, hv);
      if constexpr (d == 3) gld16_dev(hr[slot][1], hsrc + s * 1024 + 112 * 1024, hv);
    } else if constexpr (s < 36) {
      constexpr int o = s - 28;
      if constexpr (d < 2) {
        const int rb = 28 * (4 * w + (o >> 1)) + 16 * (o & 1);
        const int ra = min(rb + orow + 4 * d, 447);
        glds16(osrc, ra * 896 + chs, lb + d * 1024);
      }
      if constexpr (d == 2) gld16_dev(hr[slot][0], ofr + o * 1024, hv);
      if constexpr (d == 3) gld16_dev(hr[slot][1], ofr + o * 1024 + 16 * 2048, hv);
    }
  };
  const int hh = lane >> 5, blk = (lane >> 4) & 1, q4 = (lane & 15) >> 2, p4 = lane & 3;
  const unsigned tb_0 = (8 * hh + q4) * 128 + 16 * ((0 + 2 * blk + (p4 >> 1)) ^ (4 * ((q4 >> 1) & 1))) + 8 * (p4 & 1);
  const unsigned tb_1 = (8 * hh + q4) * 128 + 16 * ((4 + 2 * blk + (p4 >> 1)) ^ (4 * ((q4 >> 1) & 1))) + 8 * (p4 & 1);
  bf16x8 wf[2][2];
  auto ldw0 = [&](auto S) { constexpr int s = S; LAS const char* b = ring + (s % C_D) * 2048; wf[s & 1][0] = cat(ldtr(b + tb_0), ldtr(b + tb_0 + 512)); };
  auto ldw1 = [&](auto S) { constexpr int s = S; LAS const char* b = ring + (s % C_D) * 2048; wf[s & 1][1] = cat(ldtr(b + tb_1), ldtr(b + tb_1 + 512)); };
  static_for<0, C_D>([&](auto S) { issue1(S, IC<0>{}); issue1(S, IC<1>{}); });
  {
    const int l = lane & 3;
    poll_flags(ws, l < 2 ? (rt >> 1) * 14 + 6 + 2 * w + l : FLAG_G + rt, tag);
  }
  STAMPW(4);
  float g0, g1;
  asm volatile("global_load_dword %0, %2, %3 sc1\n\tglobal_load_dword %1, %2, %3 offset:128 sc1" : "=&v"(g0), "=&v"(g1) : "v"((lane & 31) * 4), "s"(ws + WS_G + tb0 * 128) : "memory");
  static_for<0, C_D>([&](auto S) { issue1(S, IC<2>{}); issue1(S, IC<3>{}); });
  ldw0(IC<0>{}); ldw1(IC<0>{});
  static_for<0, 36>([&](auto S) {
    constexpr int s = S, buf = s & 1, slot = s % C_D;
    wait_lgkm0();
    constexpr int allow = [] {
      if (s + 1 < C_D) { int n = 2 * (C_D - 1 - s); for (int t = C_D; t <= s + C_D - 1 && t < 36; ++t) n += 4; return n; }
      int n = 0; for (int t = s + 2; t <= s + C_D - 1 && t < 36; ++t) n += 4; return n; }();
    wait_vm<allow>();
    if constexpr (s == 0) STAMPW(5);
    asm volatile("" : "+v"(hr[slot][0]), "+v"(hr[slot][1]));
    const bf16x8 h0 = __builtin_bit_cast(bf16x8, hr[slot][0]), h1 = __builtin_bit_cast(bf16x8, hr[slot][1]);
    if constexpr (s == 28) {
      asm volatile("" : "+v"(g0), "+v"(g1));
      const float s0 = 1.f - g0, s1 = 1.f - g1;
#pragma unroll
      for (int ct = 0; ct < 2; ++ct) { acc[ct][0] *= s0; acc[ct][1] *= s1; }
    }
    if constexpr (s + C_D == 28) { lds_wait_ge(lds, CTL_ORDY, 1u); STAMPW(7); }
    SB();
    using SN = IC<s + C_D>;
    acc[0][0] = MFMA32(wf[buf][0], h0, acc[0][0]);
    if constexpr (s + 1 < 36) ldw0(IC<s + 1>{});
    issue1(SN{}, IC<0>{}); SB();
    acc[0][1] = MFMA32(wf[buf][0], h1, acc[0][1]);
    if constexpr (s + 1 < 36) ldw1(IC<s + 1>{});
    issue1(SN{}, IC<1>{}); SB();
    acc[1][0] = MFMA32(wf[buf][1], h0, acc[1][0]); SB();
    acc[1][1] = MFMA32(wf[buf][1], h1, acc[1][1]); SB();
    issue1(SN{}, IC<2>{});
    issue1(SN{}, IC<3>{}); SB();
    if constexpr (s == 13) STAMPW(6);
  });
  STAMPW(8);
}

__global__ __launch_bounds__(512, 2) void fused_block(const bf16* __restrict__ x, const bf16* __restrict__ wqkv, const bf16* __restrict__ wo, const bf16* __restrict__ w1,
                                                      const bf16* __restrict__ w2, const bf16* __restrict__ gw, char* __restrict__ ws, bf16* __restrict__ out) {
  __shared__ __attribute__((aligned(16))) char smem[LDS_BYTES];
  LAS char* lds = (LAS char*)smem;
  const int b = blockIdx.x, xcd = b & 7, idx = b >> 3, r = xcd >> 1, c2 = xcd & 1;
  const int tid = threadIdx.x, lane = tid & 63;
  const int w = __builtin_amdgcn_readfirstlane(tid >> 6);
  STAMP_DECL;
  STAMPW(0);
  if (tid == 0) __hip_atomic_store((GAS unsigned long long*)(ws + (40u << 20)) + b, stv.t[0], __ATOMIC_RELAXED, __HIP_MEMORY_SCOPE_AGENT);
  const bool tile = idx < 28;
  const int i = 4 * r + idx / 7, cc = idx % 7;
  if (w == 0) {
    const unsigned e = __hip_atomic_load((GAS unsigned*)(ws + WS_EPOCH), __ATOMIC_RELAXED, __HIP_MEMORY_SCOPE_AGENT);
    if (lane == 0) {
      *(LAS u32x4*)(lds + 0) = (u32x4){e * 2654435761u + 0x7F4A7C15u, e, 0u, 0u};
      *(LAS u32x4*)(lds + 16) = (u32x4){0u, 0u, 0u, 0u};
    }
    wait_lgkm0();
  }
  const char* xg = (const char*)x + (size_t)i * 128 * 896;
  const char* wg = cc < 3 ? (const char*)wqkv + (2 * cc + c2) * 448 : (const char*)w1 + (4 * c2 + cc - 3) * 448;
  const int ldw2 = cc < 3 ? NQKV * 2 : DFF * 2;
  const int rt = 8 * r + 4 * c2 + idx / 7, ctile = idx % 7;
  if (w >= 4) {
    if (tile) a_loader(lds, xg, wg, ldw2, w - 4 STAMP_ARG); else raw_barrier();
    STAMPW(2);
    b_attention(lds, ws, 4 * r + (idx >> 3), 8 * c2 + (idx & 7), w - 4, lane, tile ? (rt >> 1) : -1 STAMP_ARG);
    if (b == 0 && w == 7) {
      const unsigned tag = lds_ld(lds, CTL_TAG);
#pragma unroll
      for (int k = 0; k < 4; ++k) poll_flags(ws, k * 64 + lane, tag);
      if (lane == 0) __hip_atomic_store((GAS unsigned*)(ws + WS_EPOCH), lds_ld(lds, CTL_EPOCH) + 1u, __ATOMIC_RELAXED, __HIP_MEMORY_SCOPE_AGENT);
    }
  } else if (tile) {
    if (cc == 2) a_compute<true>(lds, 2, i, c2, w, ws, xg, wg, ldw2, cc STAMP_ARG);
    else a_compute<false>(lds, cc < 3 ? cc : 3, i, cc < 3 ? c2 : 4 * c2 + cc - 3, w, ws, xg, wg, ldw2, cc STAMP_ARG);
    publish(lds, CTL_APUB, 4u, ws, i * 14 + (cc < 3 ? 2 * cc + c2 : 6 + 4 * c2 + cc - 3), lds_ld(lds, CTL_TAG), lane);
    STAMPW(3);
    f32x16 acc[2][2] = {};
    c_gemm(lds, (const char*)w2, (const char*)wo, ws, rt, ctile, w, lane, acc STAMP_ARG);
    wait_lgkm0();
    LAS char* red = lds + RING0 + w * RING_STRIDE;
    const int hh = lane >> 5;
#pragma unroll
    for (int ct = 0; ct < 2; ++ct)
#pragma unroll
      for (int tt = 0; tt < 2; ++tt)
#pragma unroll
        for (int rg = 0; rg < 4; ++rg) {
          const f32x4 v = {acc[ct][tt][4 * rg], acc[ct][tt][4 * rg + 1], acc[ct][tt][4 * rg + 2], acc[ct][tt][4 * rg + 3]};
          *(LAS f32x4*)(red + (32 * tt + (lane & 31)) * RED_STRIDE + (32 * ct + 8 * rg + 4 * hh) * 4) = v;
        }
  } else {
    raw_barrier();
    const int gi = (idx - 28) + 4 * xcd;
    gate_rows((const char*)x, (const char*)gw, (const char*)w2, (const char*)wo, ws, gi, idx - 28, w, lane);
    publish(lds, CTL_APUB, 4u, ws, FLAG_G + gi, lds_ld(lds, CTL_TAG), lane);
    warm_w2((const char*)w2, (const char*)wo, ws, idx - 28, tid);
  }
  STAMPW(9);
  if (!tile) { STAMP_FLUSH(); return; }
  __syncthreads();
  STAMPW(10);
  {
    const int tok = tid >> 3, cg = tid & 7;
    f32x4 a = {0, 0, 0, 0}, c = {0, 0, 0, 0};
#pragma unroll
    for (int ww = 0; ww < 4; ++ww) {
      a += *(LAS const f32x4*)(lds + RING0 + ww * RING_STRIDE + tok * RED_STRIDE + cg * 32);
      c += *(LAS const f32x4*)(lds + RING0 + ww * RING_STRIDE + tok * RED_STRIDE + cg * 32 + 16);
    }
    const u32x4 v = {pk2(a[0], a[1]), pk2(a[2], a[3]), pk2(c[0], c[1]), pk2(c[2], c[3])};
    *(u32x4*)((char*)out + (size_t)(rt * 64 + tok) * 896 + ctile * 128 + cg * 16) = v;
  }
  wait_vm<0>(); STAMPW(11);
  STAMP_FLUSH();
}

extern "C" void kernel_launch(void* const* d_in, const int* in_sizes, int n_in, void* d_out, int out_size, void* d_ws, size_t ws_size, hipStream_t stream) {
  fused_block<<<256, 512, 0, stream>>>((const bf16*)d_in[0], (const bf16*)d_in[1], (const bf16*)d_in[2], (const bf16*)d_in[4], (const bf16*)d_in[5], (const bf16*)d_in[6],
                                       (char*)d_ws, (bf16*)d_out);
}
```

```cpp
#include <hip/hip_runtime.h>
#include <hip/hip_bf16.h>
#include <utility>
#include <type_traits>

typedef __hip_bfloat16 bf16;
typedef short bf16x8 __attribute__((ext_vector_type(8)));
typedef short s16x4 __attribute__((ext_vector_type(4)));
typedef short s16x2 __attribute__((ext_vector_type(2)));
typedef float f32x4 __attribute__((ext_vector_type(4)));
typedef float f32x2 __attribute__((ext_vector_type(2)));
typedef float f32x16 __attribute__((ext_vector_type(16)));
typedef unsigned u32x4 __attribute__((ext_vector_type(4)));
typedef __bf16 bf16x2_t __attribute__((ext_vector_type(2)));
typedef short v4i16_t __attribute__((ext_vector_type(4)));
#define LAS __attribute__((address_space(3)))
#define GAS __attribute__((address_space(1)))
#define DI __device__ __forceinline__
#define SB() __builtin_amdgcn_sched_barrier(0)
#define MFMA16(a, b, c) __builtin_amdgcn_mfma_f32_16x16x32_bf16(a, b, c, 0, 0, 0)
#define MFMA32(a, b, c) __builtin_amdgcn_mfma_f32_32x32x16_bf16(a, b, c, 0, 0, 0)

constexpr int NQKV = 1344, DFF = 1792;
constexpr size_t WS_FLAG  = 0;
constexpr size_t WS_EPOCH = 65536;
constexpr size_t WS_G     = 69632;
constexpr size_t WS_Q     = 131072;
constexpr size_t FRAG_TB  = 28 * 1024;
constexpr size_t WS_K     = WS_Q + 64 * FRAG_TB;
constexpr size_t WS_V     = WS_K + 64 * FRAG_TB;
constexpr size_t WS_H     = WS_V + 64 * FRAG_TB + 4096;
constexpr size_t WS_O     = WS_H + 64 * 112 * 1024;
constexpr size_t WS_END   = WS_O + 64 * 16 * 2048;
constexpr int FLAG_G = 224, FLAG_O = 256;
constexpr int CTL_TAG = 0, CTL_EPOCH = 4, CTL_APUB = 8, CTL_KV = 12, CTL_OPUB = 16, CTL_ORDY = 20, CTL_QRDY = 24;
constexpr int A_BASE = 64, A_XB = 16384, A_WB = 28672, A_SLOT = A_XB + A_WB;
constexpr int KV_K = A_BASE + A_SLOT, KV_V = KV_K + 24576;
constexpr int RING0 = KV_V + 24576, RING_STRIDE = 17392, RED_STRIDE = 272;
constexpr int LDS_BYTES = RING0 + 4 * RING_STRIDE;
constexpr int C_D = 8;
static_assert(LDS_BYTES == 163840 && C_D * 2048 <= RING_STRIDE, "lds");

#define STAMP_DECL do {} while (0)
#define STAMPW(j) do {} while (0)
#define STAMP_FLUSH() do {} while (0)
#define STAMP_PARAM
#define STAMP_ARG

template <int I, int N, class F> DI void static_for(F&& f) {
  if constexpr (I < N) { f(std::integral_constant<int, I>{}); static_for<I + 1, N>(f); }
}
template <int V> using IC = std::integral_constant<int, V>;
template <int N> DI void wait_vm() { asm volatile("s_waitcnt vmcnt(%0)" ::"n"(N) : "memory"); }
DI void wait_lgkm0() { asm volatile("s_waitcnt lgkmcnt(0)" ::: "memory"); }
DI void raw_barrier() { asm volatile("s_barrier" ::: "memory"); }
DI void glds16(const void* sbase, unsigned voff, unsigned lds_addr) {
  unsigned keep;
  asm volatile("s_mov_b32 %0, m0\n\ts_mov_b32 m0, %3\n\ts_nop 0\n\tglobal_load_lds_dwordx4 %1, %2\n\ts_mov_b32 m0, %0"
               : "=&s"(keep) : "v"(voff), "s"(sbase), "s"(lds_addr) : "memory");
}
DI void glds16_dev(const void* sbase, unsigned voff, unsigned lds_addr) {
  unsigned keep;
  asm volatile("s_mov_b32 %0, m0\n\ts_mov_b32 m0, %3\n\ts_nop 0\n\tglobal_load_lds_dwordx4 %1, %2 sc1\n\ts_mov_b32 m0, %0"
               : "=&s"(keep) : "v"(voff), "s"(sbase), "s"(lds_addr) : "memory");
}
DI void gld16_dev(u32x4& d, const void* sbase, unsigned voff) {
  asm volatile("global_load_dwordx4 %0, %1, %2 sc1" : "=v"(d) : "v"(voff), "s"(sbase) : "memory");
}
DI unsigned pk2(float a, float b) { return __builtin_bit_cast(unsigned, __builtin_convertvector((f32x2){a, b}, bf16x2_t)); }
DI unsigned relu2(unsigned x) { return __builtin_bit_cast(unsigned, __builtin_elementwise_max(__builtin_bit_cast(s16x2, x), (s16x2){0, 0})); }
DI s16x4 ldtr(LAS const char* p) { return __builtin_bit_cast(s16x4, __builtin_amdgcn_ds_read_tr16_b64_v4i16((LAS v4i16_t*)p)); }
DI bf16x8 cat(s16x4 lo, s16x4 hi) { return __builtin_shufflevector(lo, hi, 0, 1, 2, 3, 4, 5, 6, 7); }
DI unsigned lds_ld(LAS char* lds, int off) { return __hip_atomic_load((LAS unsigned*)(lds + off), __ATOMIC_RELAXED, __HIP_MEMORY_SCOPE_WORKGROUP); }
DI void lds_st(LAS char* lds, int off, unsigned v) { __hip_atomic_store((LAS unsigned*)(lds + off), v, __ATOMIC_RELAXED, __HIP_MEMORY_SCOPE_WORKGROUP); }
DI void lds_wait_ge(LAS char* lds, int off, unsigned v) {
  for (unsigned sp = 0; sp < (1u << 20) && lds_ld(lds, off) < v; ++sp) __builtin_amdgcn_s_sleep(1);
  asm volatile("" ::: "memory");
}
DI GAS unsigned* flag_ptr(char* ws, int idx) { return (GAS unsigned*)(ws + WS_FLAG) + idx * 32; }
DI void poll_flags(char* ws, int idx, unsigned tag) {
  GAS unsigned* fl = flag_ptr(ws, idx);
  for (unsigned sp = 0; sp < (1u << 18); ++sp) {
    const unsigned v = __hip_atomic_load(fl, __ATOMIC_RELAXED, __HIP_MEMORY_SCOPE_AGENT);
    if (__all(v == tag)) break;
    __builtin_amdgcn_s_sleep(2);
  }
  asm volatile("" ::: "memory");
}
DI void publish(LAS char* lds, int cnt_off, unsigned n, char* ws, int flag, unsigned tag, int lane) {
  wait_vm<0>();
  unsigned old = 0;
  if (lane == 0) old = __hip_atomic_fetch_add((LAS unsigned*)(lds + cnt_off), 1u, __ATOMIC_RELAXED, __HIP_MEMORY_SCOPE_WORKGROUP);
  old = __builtin_amdgcn_readfirstlane(old);
  if (old == n - 1 && lane == 0) __hip_atomic_store(flag_ptr(ws, flag), tag, __ATOMIC_RELAXED, __HIP_MEMORY_SCOPE_AGENT);
}

DI void a_loader(LAS char* lds, const char* xg, const char* wg, int ldw2, int w STAMP_PARAM) {
  const int lane = threadIdx.x & 63;
  const unsigned lds0 = (unsigned)(size_t)lds + A_BASE;
  unsigned xo[4], wo_[7];
#pragma unroll
  for (int tt = 0; tt < 4; ++tt) xo[tt] = (32 * w + 8 * tt + (lane >> 3)) * 896 + 16 * ((lane & 7) ^ ((lane >> 4) | (4 * (tt & 1))));
#pragma unroll
  for (int tt = 0; tt < 7; ++tt) { const int q = (7 * w + tt) * 64 + lane, rho = q / 28, cp = q % 28; wo_[tt] = rho * ldw2 + 16 * (cp ^ (2 * ((rho >> 3) & 1))); }
  auto stage = [&](int kc, int slot) {
    const char* xs = xg + kc * 128; const char* wsrc = wg + (size_t)kc * 64 * ldw2;
    const unsigned lb = lds0 + slot * A_SLOT;
#pragma unroll
    for (int tt = 0; tt < 4; ++tt) glds16(xs, xo[tt], lb + (4 * w + tt) * 1024);
#pragma unroll
    for (int tt = 0; tt < 7; ++tt) glds16(wsrc, wo_[tt], lb + A_XB + (7 * w + tt) * 1024);
  };
  stage(0, 0); stage(1, 1);
  wait_vm<11>(); raw_barrier();
  STAMPW(1);
  stage(2, 2);
  static_for<0, 6>([&](auto C) {
    constexpr int c = C;
    if constexpr (c + 2 <= 6) wait_vm<11>(); else wait_vm<0>();
    raw_barrier();
    if constexpr (c + 3 <= 6) stage(c + 3, c % 3);
  });
}

template <bool VT>
DI void a_compute(LAS char* lds0, int kind  , int i, int cgl, int w, char* ws, const char* xg, const char* wg, int ldw2, int cc STAMP_PARAM) {
  LAS char* lds = lds0 + A_BASE;
  const int lane = threadIdx.x & 63;
  const int wm = w >> 1, wn = w & 1;
  const int m = lane & 15, g = lane >> 4, q4 = (lane & 15) >> 2, p4 = lane & 3;
  const unsigned xb0 = (64 * wm + m) * 128 + 16 * ((g) ^ (m >> 1));
  const unsigned xb1 = (64 * wm + m) * 128 + 16 * ((4 + g) ^ (m >> 1));
  const unsigned wbb = A_XB + (8 * g + q4) * 448 + 8 * p4 + 7 * wn * 32;
  const unsigned wE = wbb + ((wn & 1) ? -32 * (g & 1) : 32 * (g & 1));
  const unsigned wO = wbb + ((wn & 1) ? 32 * (g & 1) : -32 * (g & 1));
  f32x4 acc[4][7];
#pragma unroll
  for (int a = 0; a < 4; ++a)
#pragma unroll
    for (int t = 0; t < 7; ++t) acc[a][t] = (f32x4){0.f, 0.f, 0.f, 0.f};
  bf16x8 xf[2][4]; s16x4 wl[2][7], wh[2][7];
  auto rd = [&](auto BUF, auto SLOT, auto KK, auto R) {
    constexpr int buf = BUF, slot = SLOT, kk = KK, r = R;
    LAS const char* base = lds + slot * A_SLOT;
    if constexpr (r < 4) xf[buf][r] = *(LAS const bf16x8*)(base + (kk ? xb1 : xb0) + r * 2048);
    else {
      constexpr int tl = (r - 4) >> 1, hi = (r - 4) & 1;
      const unsigned b = ((tl & 1) ? wO : wE) + tl * 32 + kk * 32 * 448 + hi * 4 * 448;
      if constexpr (hi) wh[buf][tl] = ldtr(base + b); else wl[buf][tl] = ldtr(base + b);
    }
  };
  auto mm = [&](auto BUF, auto J) {
    constexpr int buf = BUF, j = J, tl = j >> 2, a = j & 3;
    const bf16x8 wf = cat(wl[buf][tl], wh[buf][tl]);
    acc[a][tl] = VT ? MFMA16(xf[buf][a], wf, acc[a][tl]) : MFMA16(wf, xf[buf][a], acc[a][tl]);
  };
  raw_barrier();
  STAMPW(1);
  unsigned t0 = 0, t1 = 0;
  {
    const int il = i & 3, e = w * 64 + lane;
    const char* wlp = wg - ((size_t)wg & 64);
    const unsigned o0 = (192 + 4 * (e >> 2) + il) * ldw2 + (e & 3) * 128;
    const int xe = min(cc * 74 + (e % 74), 511);
    const unsigned o1 = (xe >> 2) * 896 + (3 + (xe & 3)) * 128;
    asm volatile("global_load_dword %0, %1, %2" : "=&v"(t0) : "v"(o0), "s"(wlp) : "memory");
    if (w < 2) asm volatile("global_load_dword %0, %1, %2" : "=&v"(t1) : "v"(o1), "s"(xg) : "memory");
  }
  static_for<0, 18>([&](auto R) { rd(IC<0>{}, IC<0>{}, IC<0>{}, R); });
  SB();
  static_for<0, 7>([&](auto C) {
    constexpr int c = C, slot = c % 3, nslot = (c + 1) % 3;
    static_for<0, 28>([&](auto J) {
      mm(IC<0>{}, J);
      if constexpr (J < 18) rd(IC<1>{}, IC<slot>{}, IC<1>{}, J);
      SB();
    });
    if constexpr (c < 6) {
      wait_lgkm0(); raw_barrier(); SB();
      static_for<0, 28>([&](auto J) {
        mm(IC<1>{}, J);
        if constexpr (J < 18) rd(IC<0>{}, IC<nslot>{}, IC<0>{}, J);
        SB();
      });
    } else {
      static_for<0, 28>([&](auto J) { mm(IC<1>{}, J); });
    }
  });
  STAMPW(2);
  wait_vm<0>(); asm volatile("" :: "v"(t0), "v"(t1));
  const auto rs = __builtin_amdgcn_make_buffer_rsrc((void*)ws, 0, (unsigned)WS_END, 0x00020000);
  const int tb0 = 4 * i + 2 * wm;
  if constexpr (!VT) {
    unsigned dst; int ntile;
    if (kind == 0) { dst = WS_Q; ntile = 28; }
    else if (kind == 1) { dst = WS_K; ntile = 28; }
    else { dst = WS_H; ntile = 112; }
    dst += (tb0 * ntile + cgl * 14 + 7 * wn) * 1024 + lane * 16;
    if (kind == 0) {
      const float sc = 0.18898223650461362f * 1.4426950408889634f;
#pragma unroll
      for (int tl = 0; tl < 7; ++tl)
#pragma unroll
        for (int a = 0; a < 4; ++a) acc[a][tl] *= sc;
    }
    const bool relu = kind == 3;
#pragma unroll
    for (int tl = 0; tl < 7; ++tl)
#pragma unroll
      for (int pr = 0; pr < 2; ++pr) {
        const f32x4 X = acc[2 * pr][tl], Y = acc[2 * pr + 1][tl];
        unsigned x0 = pk2(X[0], X[1]), x1 = pk2(X[2], X[3]), y0 = pk2(Y[0], Y[1]), y1 = pk2(Y[2], Y[3]);
        if (relu) { x0 = relu2(x0); x1 = relu2(x1); y0 = relu2(y0); y1 = relu2(y1); }
        auto r0 = __builtin_amdgcn_permlane16_swap(x0, y0, false, false);
        auto r1 = __builtin_amdgcn_permlane16_swap(x1, y1, false, false);
        __builtin_amdgcn_raw_buffer_store_b128((u32x4){r0[0], r1[0], r0[1], r1[1]}, rs, dst + (pr * ntile + tl) * 1024, 0, 16);
      }
  } else {
    const int row = lane >> 4;
    const unsigned dst = WS_V + (((2 * tb0 + (row & 1)) * 2 + (row >> 1)) * 448 + cgl * 224 + 112 * wn + (lane & 15)) * 16;
#pragma unroll
    for (int tl = 0; tl < 7; ++tl)
#pragma unroll
      for (int pr = 0; pr < 2; ++pr) {
        const f32x4 X = acc[2 * pr][tl], Y = acc[2 * pr + 1][tl];
        unsigned x0 = pk2(X[0], X[1]), x1 = pk2(X[2], X[3]), y0 = pk2(Y[0], Y[1]), y1 = pk2(Y[2], Y[3]);
        auto r0 = __builtin_amdgcn_permlane16_swap(x0, y0, false, false);
        auto r1 = __builtin_amdgcn_permlane16_swap(x1, y1, false, false);
        __builtin_amdgcn_raw_buffer_store_b128((u32x4){r0[0], r1[0], r0[1], r1[1]}, rs, dst + (pr * 4 * 448 + 16 * tl) * 16, 0, 16);
      }
  }
}

DI void gate_rows(const char* x, const char* gw, const char* w2, const char* wo, char* ws, int gi, int part4, int w, int lane) {
  const int row = gi * 64 + w * 16 + (lane >> 2), part = lane & 3;
  float s = 0.f;
#pragma unroll
  for (int k = 0; k < 14; ++k) {
    const u32x4 xv = *(const u32x4*)(x + (size_t)row * 896 + k * 64 + part * 16);
    const u32x4 gv = *(const u32x4*)(gw + k * 64 + part * 16);
#pragma unroll
    for (int e = 0; e < 4; ++e) {
      s += __uint_as_float(xv[e] << 16) * __uint_as_float(gv[e] << 16);
      s += __uint_as_float(xv[e] & 0xffff0000u) * __uint_as_float(gv[e] & 0xffff0000u);
    }
  }
  s += __shfl_xor(s, 1); s += __shfl_xor(s, 2);
  if (part == 0) __hip_atomic_store((GAS float*)(ws + WS_G) + row, 1.f / (1.f + __expf(-s)), __ATOMIC_RELAXED, __HIP_MEMORY_SCOPE_AGENT);
}
DI void warm_w2(const char* w2, const char* wo, char* ws, int part4, int t256) {
  unsigned acc = 0;
#pragma unroll
  for (int k = 0; k < 16; ++k) {
    const int e = k * 256 + t256, rr = part4 * 560 + e / 7, ln = e % 7;
    if (e < 560 * 7) {
      const char* src = rr < 1792 ? w2 + (size_t)rr * 896 : wo + (size_t)(rr - 1792) * 896;
      acc ^= *(const unsigned*)(src + ln * 128);
    }
  }
  asm volatile("" :: "v"(acc));
}

DI void b_attention(LAS char* lds, char* ws, int i, int h, int w4, int lane, int ig   STAMP_PARAM) {
  const unsigned lds0 = (unsigned)(size_t)lds;
  const unsigned tag = lds_ld(lds, CTL_TAG);
  const int u0 = (h & 1) ? (28 * h - 4) / 8 : (28 * h) / 8;
  const int hh = lane >> 5, ql = lane & 31;
  __builtin_amdgcn_s_setprio(3);
  if (w4 == 0) {
    const int hg = h >> 3, l = lane & 15;
    int f;
    if (l < 3) f = max(i - l, 0) * 14 + 2 + hg;
    else if (l < 6) f = max(i - (l - 3), 0) * 14 + 4 + hg;
    else if (l < 8) f = FLAG_G + 2 * i + (l - 6);
    else f = i * 14 + hg;
    poll_flags(ws, f, tag);
    if (lane == 0) lds_st(lds, CTL_QRDY, 1u);
  } else {
    lds_wait_ge(lds, CTL_QRDY, 1u);
  }
  STAMPW(3);
  {
    const unsigned kvo = lane * 16, vvo = (hh * 448 + 28 * h + ql) * 16;
#pragma unroll
    for (int bq = 0; bq < 3; ++bq) {
      const int bb = 3 * w4 + bq; int tb = 4 * i - 8 + bb; tb = tb < 0 ? 0 : tb;
#pragma unroll
      for (int s = 0; s < 2; ++s) {
        glds16_dev(ws + WS_K + (size_t)tb * FRAG_TB + (u0 + 2 * s) * 512, kvo, lds0 + KV_K + bb * 2048 + s * 1024);
        glds16_dev(ws + WS_V + (size_t)(2 * tb + s) * 2 * 448 * 16, vvo, lds0 + KV_V + bb * 2048 + s * 1024);
      }
    }
  }
  const int tbq = 4 * i + w4;
  u32x4 q0, q1; float gq;
  gld16_dev(q0, ws + WS_Q + (size_t)tbq * FRAG_TB + u0 * 512, lane * 16);
  gld16_dev(q1, ws + WS_Q + (size_t)tbq * FRAG_TB + (u0 + 2) * 512, lane * 16);
  asm volatile("global_load_dword %0, %1, %2 sc1" : "=v"(gq) : "v"(ql * 4), "s"(ws + WS_G + tbq * 128) : "memory");
  wait_vm<0>();
  asm volatile("" : "+v"(q0), "+v"(q1), "+v"(gq));
  if (h & 1) { if (hh == 0) { q0[0] = 0; q0[1] = 0; } } else { if (hh == 1) { q1[2] = 0; q1[3] = 0; } }
  if (ql >= 28) {
    const u32x4 ones = {0x3f803f80u, 0x3f803f80u, 0x3f803f80u, 0x3f803f80u};
#pragma unroll
    for (int bq = 0; bq < 3; ++bq)
#pragma unroll
      for (int s = 0; s < 2; ++s) *(LAS u32x4*)(lds + KV_V + (3 * w4 + bq) * 2048 + s * 1024 + lane * 16) = ones;
  }
  wait_lgkm0();
  if (lane == 0) __hip_atomic_fetch_add((LAS unsigned*)(lds + CTL_KV), 1u, __ATOMIC_RELAXED, __HIP_MEMORY_SCOPE_WORKGROUP);
  lds_wait_ge(lds, CTL_KV, 4u);
  __builtin_amdgcn_s_setprio(0);
  STAMPW(4);
  const bf16x8 qa = __builtin_bit_cast(bf16x8, q0), qb = __builtin_bit_cast(bf16x8, q1);
  const int sw = (ql & 0x13) | ((ql & 4) << 1) | ((ql & 8) >> 1);
  LAS const char* kp = lds + KV_K + w4 * 2048 + hh * 512 + sw * 16;
  LAS const char* vp = lds + KV_V + w4 * 2048 + lane * 16;
  const f32x16 zero = {};
  const float NEG = -1e30f;
  auto mask0 = [&](f32x16& s) {
#pragma unroll
    for (int r = 0; r < 16; ++r) { const int kap = (r & 7) + 16 * (r >> 3) + 8 * hh; s[r] = (kap > ql) ? s[r] : NEG; } };
  auto mask8 = [&](f32x16& s) {
#pragma unroll
    for (int r = 0; r < 16; ++r) { const int kap = (r & 7) + 16 * (r >> 3) + 8 * hh; s[r] = (kap <= ql) ? s[r] : NEG; } };
  auto kfrag = [&](int j, int s) { return *(LAS const bf16x8*)(kp + j * 2048 + s * 1024); };
  auto vfrag = [&](int j, int s) { return *(LAS const bf16x8*)(vp + j * 2048 + s * 1024); };
  auto max8 = [&](const f32x16& s, int b, float& m0, float& m1) {
    m0 = fmaxf(fmaxf(m0, s[b]), s[b + 1]); m1 = fmaxf(fmaxf(m1, s[b + 2]), s[b + 3]);
    m0 = fmaxf(fmaxf(m0, s[b + 4]), s[b + 5]); m1 = fmaxf(fmaxf(m1, s[b + 6]), s[b + 7]); };
  f32x16 o = zero;
  if (tbq >= 8) {
    float m0 = NEG, m1 = NEG;
    bf16x8 kf[3][2]; f32x16 sv[2];
    kf[0][0] = kfrag(0, 0); kf[0][1] = kfrag(0, 1); kf[1][0] = kfrag(1, 0); kf[1][1] = kfrag(1, 1);
    SB();
    static_for<0, 10>([&](auto J) {
      constexpr int j = J;
      if constexpr (j < 9) sv[j & 1] = MFMA32(kf[j % 3][0], qa, zero);
      if constexpr (j + 2 < 11) { kf[(j + 2) % 3][0] = kfrag((j + 2) % 9, 0); kf[(j + 2) % 3][1] = kfrag((j + 2) % 9, 1); }
      SB();
      if constexpr (j - 1 == 0) mask0(sv[0]);
      if constexpr (j - 1 == 8) mask8(sv[0]);
      if constexpr (j >= 1) max8(sv[(j - 1) & 1], 0, m0, m1);
      SB();
      if constexpr (j < 9) sv[j & 1] = MFMA32(kf[j % 3][1], qb, sv[j & 1]);
      SB();
      if constexpr (j >= 1) max8(sv[(j - 1) & 1], 8, m0, m1);
      SB();
    });
    float mx = fmaxf(m0, m1);
    { auto rr = __builtin_amdgcn_permlane32_swap(__float_as_uint(mx), __float_as_uint(mx), false, false);
      mx = fmaxf(__uint_as_float(rr[0]), __uint_as_float(rr[1])); }
    f32x16 negm;
#pragma unroll
    for (int r = 0; r < 16; ++r) negm[r] = -mx;
    STAMPW(5);
    bf16x8 vf[2][2];
    vf[0][0] = vfrag(0, 0); vf[0][1] = vfrag(0, 1);
    kf[2][0] = kfrag(2, 0); kf[2][1] = kfrag(2, 1);
    sv[0] = MFMA32(kf[0][0], qa, negm); sv[0] = MFMA32(kf[0][1], qb, sv[0]);
    SB();
    static_for<0, 9>([&](auto J) {
      constexpr int j = J;
      f32x16& S = sv[j & 1];
      u32x4 pa, pb;
      if constexpr (j + 1 < 9) sv[(j + 1) & 1] = MFMA32(kf[(j + 1) % 3][0], qa, negm);
      if constexpr (j + 1 < 9) { vf[(j + 1) & 1][0] = vfrag(j + 1, 0); vf[(j + 1) & 1][1] = vfrag(j + 1, 1); }
      SB();
      if constexpr (j == 0) mask0(sv[0]);
      if constexpr (j == 8) mask8(sv[0]);
      pa[0] = pk2(__builtin_amdgcn_exp2f(S[0]), __builtin_amdgcn_exp2f(S[1]));
      pa[1] = pk2(__builtin_amdgcn_exp2f(S[2]), __builtin_amdgcn_exp2f(S[3]));
      SB();
      if constexpr (j + 1 < 9) sv[(j + 1) & 1] = MFMA32(kf[(j + 1) % 3][1], qb, sv[(j + 1) & 1]);
      if constexpr (j + 3 < 9) { kf[j % 3][0] = kfrag(j + 3, 0); kf[j % 3][1] = kfrag(j + 3, 1); }
      SB();
      pa[2] = pk2(__builtin_amdgcn_exp2f(S[4]), __builtin_amdgcn_exp2f(S[5]));
      pa[3] = pk2(__builtin_amdgcn_exp2f(S[6]), __builtin_amdgcn_exp2f(S[7]));
      pb[0] = pk2(__builtin_amdgcn_exp2f(S[8]), __builtin_amdgcn_exp2f(S[9]));
      SB();
      o = MFMA32(vf[j & 1][0], __builtin_bit_cast(bf16x8, pa), o);
      SB();
      pb[1] = pk2(__builtin_amdgcn_exp2f(S[10]), __builtin_amdgcn_exp2f(S[11]));
      pb[2] = pk2(__builtin_amdgcn_exp2f(S[12]), __builtin_amdgcn_exp2f(S[13]));
      pb[3] = pk2(__builtin_amdgcn_exp2f(S[14]), __builtin_amdgcn_exp2f(S[15]));
      SB();
      o = MFMA32(vf[j & 1][1], __builtin_bit_cast(bf16x8, pb), o);
      SB();
    });
  } else {
    const int j0 = 8 - tbq;
    float m0 = NEG, m1 = NEG;
    for (int j = j0; j < 9; ++j) {
      f32x16 s = MFMA32(kfrag(j, 0), qa, zero); s = MFMA32(kfrag(j, 1), qb, s);
      if (j == 8) mask8(s);
      max8(s, 0, m0, m1); max8(s, 8, m0, m1);
    }
    float mx = fmaxf(m0, m1);
    { auto rr = __builtin_amdgcn_permlane32_swap(__float_as_uint(mx), __float_as_uint(mx), false, false);
      mx = fmaxf(__uint_as_float(rr[0]), __uint_as_float(rr[1])); }
    f32x16 negm;
#pragma unroll
    for (int r = 0; r < 16; ++r) negm[r] = -mx;
    for (int j = j0; j < 9; ++j) {
      f32x16 s = MFMA32(kfrag(j, 0), qa, negm); s = MFMA32(kfrag(j, 1), qb, s);
      if (j == 8) mask8(s);
      u32x4 pa, pb;
#pragma unroll
      for (int r = 0; r < 4; ++r) { pa[r] = pk2(__builtin_amdgcn_exp2f(s[2 * r]), __builtin_amdgcn_exp2f(s[2 * r + 1])); pb[r] = pk2(__builtin_amdgcn_exp2f(s[8 + 2 * r]), __builtin_amdgcn_exp2f(s[9 + 2 * r])); }
      o = MFMA32(vfrag(j, 0), __builtin_bit_cast(bf16x8, pa), o); o = MFMA32(vfrag(j, 1), __builtin_bit_cast(bf16x8, pb), o);
    }
  }
  STAMPW(6);
  __builtin_amdgcn_s_setprio(3);
  float l;
  { auto rr = __builtin_amdgcn_permlane32_swap(__float_as_uint(o[12]), __float_as_uint(o[12]), false, false); l = __uint_as_float(rr[1]); }
  const float scl = gq / l;
  u32x4 f0, f1;
  f0[0] = pk2(o[0] * scl, o[1] * scl); f0[1] = pk2(o[2] * scl, o[3] * scl); f0[2] = pk2(o[4] * scl, o[5] * scl); f0[3] = pk2(o[6] * scl, o[7] * scl);
  f1[0] = pk2(o[8] * scl, o[9] * scl); f1[1] = pk2(o[10] * scl, o[11] * scl); f1[2] = pk2(o[12] * scl, o[13] * scl); f1[3] = pk2(o[14] * scl, o[15] * scl);
  if (hh) { f1[2] = 0; f1[3] = 0; }
  {
    const auto rs = __builtin_amdgcn_make_buffer_rsrc((void*)ws, 0, (unsigned)WS_END, 0x00020000);
    const unsigned off = WS_O + (tbq * 16 + h) * 2048 + lane * 16;
    __builtin_amdgcn_raw_buffer_store_b128(f0, rs, off, 0, 16);
    __builtin_amdgcn_raw_buffer_store_b128(f1, rs, off + 1024, 0, 16);
  }
  publish(lds, CTL_OPUB, 4u, ws, FLAG_O + i * 16 + h, tag, lane);
  STAMPW(7);
  if (w4 == 0 && ig >= 0) {
    poll_flags(ws, FLAG_O + ig * 16 + (lane & 15), tag);
    if (lane == 0) lds_st(lds, CTL_ORDY, 1u);
    STAMPW(8);
  }
}

DI void c_gemm(LAS char* lds, const char* w2, const char* wo, char* ws, int rt, int ctile, int w, int lane, f32x16 (&acc)[2][2] STAMP_PARAM) {
  const unsigned ring0 = (unsigned)(size_t)lds + RING0 + w * RING_STRIDE;
  LAS char* ring = lds + RING0 + w * RING_STRIDE;
  const unsigned tag = lds_ld(lds, CTL_TAG);
  const int tb0 = 2 * rt;
  const char* hsrc = ws + WS_H + ((size_t)tb0 * 112 + 28 * w) * 1024;
  const char* ofr = ws + WS_O + ((size_t)tb0 * 16 + 4 * w) * 2048;
  const char* wsrc = w2 + (size_t)(448 * w) * 896 + ctile * 128;
  const char* osrc = wo + ctile * 128;
  const unsigned hv = lane * 16;
  const unsigned chs = 16 * ((lane & 7) ^ (4 * ((lane >> 4) & 1)));
  const unsigned wv = (lane >> 3) * 896 + chs;
  const int orow = ((lane >> 3) & 3) + 8 * (lane >> 5);
  u32x4 hr[C_D][2];
  auto issue1 = [&](auto S, auto Dn) {
    constexpr int s = S, d = Dn, slot = s % C_D;
    const unsigned lb = ring0 + slot * 2048;
    if constexpr (s < 28) {
      if constexpr (d == 0) glds16(wsrc + (size_t)(16 * s) * 896, wv, lb);
      if constexpr (d == 1) glds16(wsrc + (size_t)(16 * s + 8) * 896, wv, lb + 1024);
      if constexpr (d == 2) gld16_dev(hr[slot][0], hsrc + s * 1024, hv);
      if constexpr (d == 3) gld16_dev(hr[slot][1], hsrc + s * 1024 + 112 * 1024, hv);
    } else if constexpr (s < 36) {
      constexpr int o = s - 28;
      if constexpr (d < 2) {
        const int rb = 28 * (4 * w + (o >> 1)) + 16 * (o & 1);
        const int ra = min(rb + orow + 4 * d, 447);
        glds16(osrc, ra * 896 + chs, lb + d * 1024);
      }
      if constexpr (d == 2) gld16_dev(hr[slot][0], ofr + o * 1024, hv);
      if constexpr (d == 3) gld16_dev(hr[slot][1], ofr + o * 1024 + 16 * 2048, hv);
    }
  };
  const int hh = lane >> 5, blk = (lane >> 4) & 1, q4 = (lane & 15) >> 2, p4 = lane & 3;
  const unsigned tb_0 = (8 * hh + q4) * 128 + 16 * ((0 + 2 * blk + (p4 >> 1)) ^ (4 * ((q4 >> 1) & 1))) + 8 * (p4 & 1);
  const unsigned tb_1 = (8 * hh + q4) * 128 + 16 * ((4 + 2 * blk + (p4 >> 1)) ^ (4 * ((q4 >> 1) & 1))) + 8 * (p4 & 1);
  bf16x8 wf[2][2];
  auto ldw0 = [&](auto S) { constexpr int s = S; LAS const char* b = ring + (s % C_D) * 2048; wf[s & 1][0] = cat(ldtr(b + tb_0), ldtr(b + tb_0 + 512)); };
  auto ldw1 = [&](auto S) { constexpr int s = S; LAS const char* b = ring + (s % C_D) * 2048; wf[s & 1][1] = cat(ldtr(b + tb_1), ldtr(b + tb_1 + 512)); };
  static_for<0, C_D>([&](auto S) { issue1(S, IC<0>{}); issue1(S, IC<1>{}); });
  {
    const int l = lane & 3;
    poll_flags(ws, l < 2 ? (rt >> 1) * 14 + 6 + 2 * w + l : FLAG_G + rt, tag);
  }
  STAMPW(4);
  float g0, g1;
  asm volatile("global_load_dword %0, %2, %3 sc1\n\tglobal_load_dword %1, %2, %3 offset:128 sc1" : "=&v"(g0), "=&v"(g1) : "v"((lane & 31) * 4), "s"(ws + WS_G + tb0 * 128) : "memory");
  static_for<0, C_D>([&](auto S) { issue1(S, IC<2>{}); issue1(S, IC<3>{}); });
  ldw0(IC<0>{}); ldw1(IC<0>{});
  static_for<0, 36>([&](auto S) {
    constexpr int s = S, buf = s & 1, slot = s % C_D;
    wait_lgkm0();
    constexpr int allow = [] {
      if (s + 1 < C_D) { int n = 2 * (C_D - 1 - s); for (int t = C_D; t <= s + C_D - 1 && t < 36; ++t) n += 4; return n; }
      int n = 0; for (int t = s + 2; t <= s + C_D - 1 && t < 36; ++t) n += 4; return n; }();
    wait_vm<allow>();
    if constexpr (s == 0) STAMPW(5);
    asm volatile("" : "+v"(hr[slot][0]), "+v"(hr[slot][1]));
    const bf16x8 h0 = __builtin_bit_cast(bf16x8, hr[slot][0]), h1 = __builtin_bit_cast(bf16x8, hr[slot][1]);
    if constexpr (s == 28) {
      asm volatile("" : "+v"(g0), "+v"(g1));
      const float s0 = 1.f - g0, s1 = 1.f - g1;
#pragma unroll
      for (int ct = 0; ct < 2; ++ct) { acc[ct][0] *= s0; acc[ct][1] *= s1; }
    }
    if constexpr (s + C_D == 28) { lds_wait_ge(lds, CTL_ORDY, 1u); STAMPW(7); }
    SB();
    using SN = IC<s + C_D>;
    acc[0][0] = MFMA32(wf[buf][0], h0, acc[0][0]);
    if constexpr (s + 1 < 36) ldw0(IC<s + 1>{});
    issue1(SN{}, IC<0>{}); SB();
    acc[0][1] = MFMA32(wf[buf][0], h1, acc[0][1]);
    if constexpr (s + 1 < 36) ldw1(IC<s + 1>{});
    issue1(SN{}, IC<1>{}); SB();
    acc[1][0] = MFMA32(wf[buf][1], h0, acc[1][0]); SB();
    acc[1][1] = MFMA32(wf[buf][1], h1, acc[1][1]); SB();
    issue1(SN{}, IC<2>{});
    issue1(SN{}, IC<3>{}); SB();
  });
  STAMPW(8);
}

__global__ __launch_bounds__(512, 2) void fused_block(const bf16* __restrict__ x, const bf16* __restrict__ wqkv, const bf16* __restrict__ wo, const bf16* __restrict__ w1,
                                                      const bf16* __restrict__ w2, const bf16* __restrict__ gw, char* __restrict__ ws, bf16* __restrict__ out) {
  __shared__ __attribute__((aligned(16))) char smem[LDS_BYTES];
  LAS char* lds = (LAS char*)smem;
  const int b = blockIdx.x, xcd = b & 7, idx = b >> 3, r = xcd >> 1, c2 = xcd & 1;
  const int tid = threadIdx.x, lane = tid & 63;
  const int w = __builtin_amdgcn_readfirstlane(tid >> 6);
  STAMP_DECL;
  STAMPW(0);
  const bool tile = idx < 28;
  const int i = 4 * r + idx / 7, cc = idx % 7;
  if (w == 0) {
    const unsigned e = __hip_atomic_load((GAS unsigned*)(ws + WS_EPOCH), __ATOMIC_RELAXED, __HIP_MEMORY_SCOPE_AGENT);
    if (lane == 0) {
      *(LAS u32x4*)(lds + 0) = (u32x4){e * 2654435761u + 0x7F4A7C15u, e, 0u, 0u};
      *(LAS u32x4*)(lds + 16) = (u32x4){0u, 0u, 0u, 0u};
    }
    wait_lgkm0();
  }
  const char* xg = (const char*)x + (size_t)i * 128 * 896;
  const char* wg = cc < 3 ? (const char*)wqkv + (2 * cc + c2) * 448 : (const char*)w1 + (4 * c2 + cc - 3) * 448;
  const int ldw2 = cc < 3 ? NQKV * 2 : DFF * 2;
  const int rt = 8 * r + 4 * c2 + idx / 7, ctile = idx % 7;
  if (w >= 4) {
    if (tile) a_loader(lds, xg, wg, ldw2, w - 4 STAMP_ARG); else raw_barrier();
    STAMPW(2);
    b_attention(lds, ws, 4 * r + (idx >> 3), 8 * c2 + (idx & 7), w - 4, lane, tile ? (rt >> 1) : -1 STAMP_ARG);
    if (b == 0 && w == 7) {
      const unsigned tag = lds_ld(lds, CTL_TAG);
#pragma unroll
      for (int k = 0; k < 4; ++k) poll_flags(ws, k * 64 + lane, tag);
      if (lane == 0) __hip_atomic_store((GAS unsigned*)(ws + WS_EPOCH), lds_ld(lds, CTL_EPOCH) + 1u, __ATOMIC_RELAXED, __HIP_MEMORY_SCOPE_AGENT);
    }
  } else if (tile) {
    if (cc == 2) a_compute<true>(lds, 2, i, c2, w, ws, xg, wg, ldw2, cc STAMP_ARG);
    else a_compute<false>(lds, cc < 3 ? cc : 3, i, cc < 3 ? c2 : 4 * c2 + cc - 3, w, ws, xg, wg, ldw2, cc STAMP_ARG);
    publish(lds, CTL_APUB, 4u, ws, i * 14 + (cc < 3 ? 2 * cc + c2 : 6 + 4 * c2 + cc - 3), lds_ld(lds, CTL_TAG), lane);
    STAMPW(3);
    f32x16 acc[2][2] = {};
    c_gemm(lds, (const char*)w2, (const char*)wo, ws, rt, ctile, w, lane, acc STAMP_ARG);
    wait_lgkm0();
    LAS char* red = lds + RING0 + w * RING_STRIDE;
    const int hh = lane >> 5;
#pragma unroll
    for (int ct = 0; ct < 2; ++ct)
#pragma unroll
      for (int tt = 0; tt < 2; ++tt)
#pragma unroll
        for (int rg = 0; rg < 4; ++rg) {
          const f32x4 v = {acc[ct][tt][4 * rg], acc[ct][tt][4 * rg + 1], acc[ct][tt][4 * rg + 2], acc[ct][tt][4 * rg + 3]};
          *(LAS f32x4*)(red + (32 * tt + (lane & 31)) * RED_STRIDE + (32 * ct + 8 * rg + 4 * hh) * 4) = v;
        }
  } else {
    raw_barrier();
    const int gi = (idx - 28) + 4 * xcd;
    gate_rows((const char*)x, (const char*)gw, (const char*)w2, (const char*)wo, ws, gi, idx - 28, w, lane);
    publish(lds, CTL_APUB, 4u, ws, FLAG_G + gi, lds_ld(lds, CTL_TAG), lane);
    warm_w2((const char*)w2, (const char*)wo, ws, idx - 28, tid);
  }
  STAMPW(9);
  if (!tile) { STAMP_FLUSH(); return; }
  __syncthreads();
  STAMPW(10);
  {
    const int tok = tid >> 3, cg = tid & 7;
    f32x4 a = {0, 0, 0, 0}, c = {0, 0, 0, 0};
#pragma unroll
    for (int ww = 0; ww < 4; ++ww) {
      a += *(LAS const f32x4*)(lds + RING0 + ww * RING_STRIDE + tok * RED_STRIDE + cg * 32);
      c += *(LAS const f32x4*)(lds + RING0 + ww * RING_STRIDE + tok * RED_STRIDE + cg * 32 + 16);
    }
    const u32x4 v = {pk2(a[0], a[1]), pk2(a[2], a[3]), pk2(c[0], c[1]), pk2(c[2], c[3])};
    { const auto ro = __builtin_amdgcn_make_buffer_rsrc((void*)out, 0, 2048 * 896, 0x00020000);
      __builtin_amdgcn_raw_buffer_store_b128(v, ro, (rt * 64 + tok) * 896 + ctile * 128 + cg * 16, 0, 2); }
  }
  STAMP_FLUSH();
}

extern "C" void kernel_launch(void* const* d_in, const int* in_sizes, int n_in, void* d_out, int out_size, void* d_ws, size_t ws_size, hipStream_t stream) {
  fused_block<<<256, 512, 0, stream>>>((const bf16*)d_in[0], (const bf16*)d_in[1], (const bf16*)d_in[2], (const bf16*)d_in[4], (const bf16*)d_in[5], (const bf16*)d_in[6],
                                       (char*)d_ws, (bf16*)d_out);
}
```

```cpp
#define PUB_CHUNK 4
#define C_PRE 6
#define CODEWARM 44800
#include <hip/hip_runtime.h>
#include <hip/hip_bf16.h>
#include <utility>
#include <type_traits>

typedef __hip_bfloat16 bf16;
typedef short bf16x8 __attribute__((ext_vector_type(8)));
typedef short s16x4 __attribute__((ext_vector_type(4)));
typedef short s16x2 __attribute__((ext_vector_type(2)));
typedef float f32x4 __attribute__((ext_vector_type(4)));
typedef float f32x2 __attribute__((ext_vector_type(2)));
typedef float f32x16 __attribute__((ext_vector_type(16)));
typedef unsigned u32x4 __attribute__((ext_vector_type(4)));
typedef __bf16 bf16x2_t __attribute__((ext_vector_type(2)));
typedef short v4i16_t __attribute__((ext_vector_type(4)));
#define LAS __attribute__((address_space(3)))
#define GAS __attribute__((address_space(1)))
#define DI __device__ __forceinline__
#define SB() __builtin_amdgcn_sched_barrier(0)
#define MFMA16(a, b, c) __builtin_amdgcn_mfma_f32_16x16x32_bf16(a, b, c, 0, 0, 0)
#define MFMA32(a, b, c) __builtin_amdgcn_mfma_f32_32x32x16_bf16(a, b, c, 0, 0, 0)

constexpr int NQKV = 1344, DFF = 1792;
constexpr size_t WS_FLAG  = 0;
constexpr size_t WS_EPOCH = 98304;
constexpr size_t WS_G     = 102400;
constexpr size_t WS_QKV   = 131072;
constexpr size_t QKV_TB   = 84 * 1024;
constexpr size_t WS_H     = WS_QKV + 64 * QKV_TB;
constexpr size_t WS_O     = WS_H + 64 * 112 * 1024;
constexpr size_t WS_END   = WS_O + 64 * 16 * 2048;
constexpr int FLAG_H = 224, FLAG_G = 448, FLAG_O = 512;
constexpr int CTL_TAG = 0, CTL_EPOCH = 4, CTL_APUB0 = 8, CTL_KV = 12, CTL_OPUB = 16, CTL_ORDY = 20, CTL_QRDY = 24, CTL_APUB1 = 28, CTL_CDONE = 32, CTL_RDY = 36  ;
constexpr int CTL_RING = 48;
constexpr int A_X = 0, A_W = 7 * 16384;
constexpr int LDS_BYTES = 163840;
constexpr int KV_K = 114688, KV_V = KV_K + 24576;
constexpr int RING0 = 64, RING_STRIDE = 28656, RED_STRIDE = 272;
#define C_DEPTH 13
#define W2_Q0 0
#define W2_Q1 2
#define W2_Q2 4
#define W2_Q3 6
#define W2_Q4 9
#define W2_Q5 16
#define POLL_SLEEP 1
#define A_VSTAGE 0
#define MALL_DELAY 0
#define H_DELAY 3
#define C_HD 8
constexpr int C_D = C_DEPTH;
constexpr int C_H = C_HD;
constexpr int C_P = C_PRE;
static_assert(C_D * 2048 <= RING_STRIDE && RING0 + 4 * RING_STRIDE <= KV_K && KV_V + 24576 == LDS_BYTES, "lds");

#define STAMP_DECL do {} while (0)
#define STAMPW(j) do {} while (0)
#define STAMP_FLUSH() do {} while (0)
#define STAMP_PARAM
#define STAMP_ARG

template <int I, int N, class F> DI void static_for(F&& f) {
  if constexpr (I < N) { f(std::integral_constant<int, I>{}); static_for<I + 1, N>(f); }
}
template <int V> using IC = std::integral_constant<int, V>;
template <int N> DI void wait_vm() { asm volatile("s_waitcnt vmcnt(%0)" ::"n"(N) : "memory"); }
DI void wait_lgkm0() { asm volatile("s_waitcnt lgkmcnt(0)" ::: "memory"); }
DI void raw_barrier() { asm volatile("s_barrier" ::: "memory"); }
DI void glds16(const void* sbase, unsigned voff, unsigned lds_addr) {
  unsigned keep;
  asm volatile("s_mov_b32 %0, m0\n\ts_mov_b32 m0, %3\n\ts_nop 0\n\tglobal_load_lds_dwordx4 %1, %2\n\ts_mov_b32 m0, %0"
               : "=&s"(keep) : "v"(voff), "s"(sbase), "s"(lds_addr) : "memory");
}
DI void glds16_dev(const void* sbase, unsigned voff, unsigned lds_addr) {
  unsigned keep;
  asm volatile("s_mov_b32 %0, m0\n\ts_mov_b32 m0, %3\n\ts_nop 0\n\tglobal_load_lds_dwordx4 %1, %2 sc1\n\ts_mov_b32 m0, %0"
               : "=&s"(keep) : "v"(voff), "s"(sbase), "s"(lds_addr) : "memory");
}
DI void gld16(u32x4& d, const void* sbase, unsigned voff) {
  asm volatile("global_load_dwordx4 %0, %1, %2" : "=v"(d) : "v"(voff), "s"(sbase) : "memory");
}
DI void gld16_dev(u32x4& d, const void* sbase, unsigned voff) {
  asm volatile("global_load_dwordx4 %0, %1, %2 sc1" : "=v"(d) : "v"(voff), "s"(sbase) : "memory");
}
DI unsigned pk2(float a, float b) { return __builtin_bit_cast(unsigned, __builtin_convertvector((f32x2){a, b}, bf16x2_t)); }
DI unsigned relu2(unsigned x) { return __builtin_bit_cast(unsigned, __builtin_elementwise_max(__builtin_bit_cast(s16x2, x), (s16x2){0, 0})); }
DI s16x4 ldtr(LAS const char* p) { return __builtin_bit_cast(s16x4, __builtin_amdgcn_ds_read_tr16_b64_v4i16((LAS v4i16_t*)p)); }
DI bf16x8 cat(s16x4 lo, s16x4 hi) { return __builtin_shufflevector(lo, hi, 0, 1, 2, 3, 4, 5, 6, 7); }
DI unsigned lds_ld(LAS char* lds, int off) { return __hip_atomic_load((LAS unsigned*)(lds + off), __ATOMIC_RELAXED, __HIP_MEMORY_SCOPE_WORKGROUP); }
DI void lds_st(LAS char* lds, int off, unsigned v) { __hip_atomic_store((LAS unsigned*)(lds + off), v, __ATOMIC_RELAXED, __HIP_MEMORY_SCOPE_WORKGROUP); }
DI void lds_wait_ge(LAS char* lds, int off, unsigned v) {
  for (unsigned sp = 0; sp < (1u << 20) && lds_ld(lds, off) < v; ++sp) __builtin_amdgcn_s_sleep(1);
  asm volatile("" ::: "memory");
}
DI GAS unsigned* flag_ptr(char* ws, int idx) { return (GAS unsigned*)(ws + WS_FLAG) + idx * 32; }
DI void poll_flags(char* ws, int idx, unsigned tag) {
  GAS unsigned* fl = flag_ptr(ws, idx);
  for (unsigned sp = 0; sp < (1u << 18); ++sp) {
    const unsigned v = __hip_atomic_load(fl, __ATOMIC_RELAXED, __HIP_MEMORY_SCOPE_AGENT);
    if (__all(v == tag)) break;
    __builtin_amdgcn_s_sleep(POLL_SLEEP);
  }
  asm volatile("" ::: "memory");
}
DI void publish(LAS char* lds, int cnt_off, unsigned n, char* ws, int flag, unsigned tag, int lane) {
  wait_vm<0>();
  unsigned old = 0;
  if (lane == 0) old = __hip_atomic_fetch_add((LAS unsigned*)(lds + cnt_off), 1u, __ATOMIC_RELAXED, __HIP_MEMORY_SCOPE_WORKGROUP);
  old = __builtin_amdgcn_readfirstlane(old);
  if (old == n - 1 && lane == 0) __hip_atomic_store(flag_ptr(ws, flag), tag, __ATOMIC_RELAXED, __HIP_MEMORY_SCOPE_AGENT);
}

DI void put_tag(LAS char* lds, unsigned e, int lane) {
  if (lane == 0) {
    *(LAS u32x4*)(lds + 0) = (u32x4){e * 2654435761u + 0x7F4A7C15u, e, 0u, 0u};
    *(LAS u32x4*)(lds + 16) = (u32x4){0u, 0u, 0u, 0u};
    *(LAS u32x4*)(lds + 32) = (u32x4){0u, 0u, 0u, 0u};
    *(LAS u32x4*)(lds + 48) = (u32x4){0u, 0u, 0u, 0u};
  }
  wait_lgkm0();
}
DI void set_tag(LAS char* lds, char* ws, int lane) {
  put_tag(lds, __hip_atomic_load((GAS unsigned*)(ws + WS_EPOCH), __ATOMIC_RELAXED, __HIP_MEMORY_SCOPE_AGENT), lane);
}

#define FFIRST 0
template <int PASS> struct PassCfg { static constexpr bool F = (PASS == 0) == (FFIRST != 0); static constexpr int NL = F ? 4 : 3, NT = F ? 4 : 3, ROWB = F ? 256 : 192; };

DI void a_loader(LAS char* lds, const char* xg, const char* wf  , const char* wq  , int w STAMP_PARAM) {
  const int lane = threadIdx.x & 63;
  const unsigned lds0 = (unsigned)(size_t)lds;
  unsigned xo[4], gf[4], gq[3];
#pragma unroll
  for (int tt = 0; tt < 4; ++tt) xo[tt] = (32 * w + 8 * tt + (lane >> 3)) * 896 + 16 * ((lane & 7) ^ ((lane >> 4) | (4 * (tt & 1))));
  auto ldx = [&](int kc) {
#pragma unroll
    for (int tt = 0; tt < 4; ++tt) glds16(xg + kc * 128, xo[tt], lds0 + A_X + kc * 16384 + (4 * w + tt) * 1024);
  };
  ldx(0);
  auto mkf = [&] {
#pragma unroll
    for (int tt = 0; tt < 4; ++tt) { const int rho = 4 * (4 * w + tt) + (lane >> 4), cp = lane & 15; gf[tt] = rho * (DFF * 2) + 16 * (cp ^ (2 * ((rho & 3) | (((rho >> 3) & 1) << 2)))); } };
  auto mkq = [&] {
#pragma unroll
    for (int tt = 0; tt < 3; ++tt) { const int q = (3 * w + tt) * 64 + lane, rho = q / 12, cp = q % 12; gq[tt] = rho * (NQKV * 2) + 16 * (cp ^ (2 * ((rho >> 3) & 1))); } };
  if constexpr (FFIRST) mkf(); else mkq();
  auto ldw = [&](auto N) {
    constexpr int n = N, kc = n % 7; using P = PassCfg<n / 7>;
#pragma unroll
    for (int tt = 0; tt < P::NL; ++tt) {
      if constexpr (P::F) glds16(wf + (size_t)kc * 64 * (DFF * 2), gf[tt], lds0 + A_W + (n % 3) * 16384 + (4 * w + tt) * 1024);
      else glds16(wq + (size_t)kc * 64 * (NQKV * 2), gq[tt], lds0 + A_W + (n % 3) * 16384 + (3 * w + tt) * 1024);
    }
  };
  ldw(IC<0>{}); ldx(1); ldw(IC<1>{});
  wait_vm<4 + PassCfg<0>::NL>(); raw_barrier();
  STAMPW(1);
  ldx(2); ldw(IC<2>{}); ldx(3);
  if constexpr (FFIRST) mkq(); else mkf();
  static_for<0, 13>([&](auto N) {
    constexpr int n = N;
    constexpr int allow = (n + 2 <= 6 ? 4 : 0) + (n + 2 > 13 ? 0 : (n + 2 < 7 ? PassCfg<0>::NL : PassCfg<1>::NL)) + (n + 3 <= 6 ? 4 : 0);
    wait_vm<allow>(); raw_barrier();
    if constexpr (n + 3 <= 13) ldw(IC<n + 3>{});
    if constexpr (n + 4 <= 6) ldx(n + 4);
  });
}

template <int PASS, class ACC, class HOOK>
DI void a_pass(LAS char* lds, ACC& acc, bf16x8 (&xf)[2][4], s16x4 (&wl)[2][4], s16x4 (&wh)[2][4], const unsigned (&xb)[2], const unsigned (&wb)[4], const unsigned (&wbn)[4], HOOK&& hook) {
  using P = PassCfg<PASS>; using PN = PassCfg<1>;
  constexpr int NT = P::NT, NM = 4 * NT;
  auto rd = [&](auto BUF, auto N, auto KK, auto R, auto NEXTP) {
    constexpr int buf = BUF, n = N, kk = KK, r = R; constexpr bool nextp = NEXTP;
    constexpr int rowb = PassCfg<n / 7>::ROWB;
    if constexpr (r < 4) xf[buf][r] = *(LAS const bf16x8*)(lds + A_X + (n % 7) * 16384 + xb[kk] + r * 2048);
    else {
      constexpr int tl = (r - 4) >> 1, hi = (r - 4) & 1;
      LAS const char* p = lds + A_W + (n % 3) * 16384 + (nextp ? wbn[tl] : wb[tl]) + (kk * 32 + hi * 4) * rowb;
      if constexpr (hi) wh[buf][tl] = ldtr(p); else wl[buf][tl] = ldtr(p);
    }
  };
  auto mm = [&](auto BUF, auto J) {
    constexpr int buf = BUF, j = J, tl = j >> 2, a = j & 3;
    acc[a][tl] = MFMA16(cat(wl[buf][tl], wh[buf][tl]), xf[buf][a], acc[a][tl]);
  };
  static_for<0, 7>([&](auto C) {
    constexpr int n = 7 * PASS + C;
    static_for<0, NM>([&](auto J) {
      mm(IC<0>{}, J);
      if constexpr (J < 4 + 2 * NT) rd(IC<1>{}, IC<n>{}, IC<1>{}, J, IC<0>{});
      SB();
    });
    if constexpr (n < 13) {
      wait_lgkm0(); raw_barrier(); hook(C); SB();
      constexpr bool np = (n + 1) / 7 != PASS;
      constexpr int NR = 4 + 2 * (np ? PN::NT : NT);
      static_for<0, NM>([&](auto J) {
        mm(IC<1>{}, J);
        if constexpr (J < NR) rd(IC<0>{}, IC<n + 1>{}, IC<0>{}, J, IC<np>{});
        SB();
      });
    } else {
      static_for<0, NM>([&](auto J) { mm(IC<1>{}, J); SB(); });
    }
  });
}

template <bool F, int NT>
DI void a_store(f32x4 (&acc)[4][NT], char* ws, int tb0, int tile0  , int lane) {
  const auto rs = __builtin_amdgcn_make_buffer_rsrc((void*)ws, 0, (unsigned)WS_END, 0x00020000);
  constexpr int ntile = F ? 112 : 84;
  const unsigned dst = (F ? WS_H : WS_QKV) + (tb0 * ntile + tile0) * 1024 + lane * 16;
#pragma unroll
  for (int tl = 0; tl < NT; ++tl) {
    const float sc = (!F && tile0 + tl < 28) ? 0.18898223650461362f * 1.4426950408889634f : 1.f;
#pragma unroll
    for (int pr = 0; pr < 2; ++pr) {
      f32x4 X = acc[2 * pr][tl], Y = acc[2 * pr + 1][tl];
      if constexpr (!F) { X *= sc; Y *= sc; }
      unsigned x0 = pk2(X[0], X[1]), x1 = pk2(X[2], X[3]), y0 = pk2(Y[0], Y[1]), y1 = pk2(Y[2], Y[3]);
      if constexpr (F) { x0 = relu2(x0); x1 = relu2(x1); y0 = relu2(y0); y1 = relu2(y1); }
      auto r0 = __builtin_amdgcn_permlane16_swap(x0, y0, false, false);
      auto r1 = __builtin_amdgcn_permlane16_swap(x1, y1, false, false);
      __builtin_amdgcn_raw_buffer_store_b128((u32x4){r0[0], r1[0], r0[1], r1[1]}, rs, dst + (pr * ntile + tl) * 1024, 0, 16);
    }
  }
}

DI const char* warm_line(int L, const char* x, const char* wqkv, const char* w1, const char* w2, const char* wo) {
  if (L < 10240) return x + (size_t)(L / 5) * 896 + (2 + L % 5) * 128;
  L -= 10240;
  if (L < 6720) return wqkv + (size_t)(128 + L / 21) * 2688 + (L % 21) * 128;
  L -= 6720;
  if (L < 12544) return w1 + (size_t)L * 128;
  L -= 12544;
  if (L < 12544) return w2 + (size_t)L * 128;
  L -= 12544;
  return wo + (size_t)min(L, 3135) * 128;
}
DI void a_compute(LAS char* lds, int i, int c, int w, char* ws, const char* warm STAMP_PARAM) {
  const int lane = threadIdx.x & 63;
  unsigned epoch, wdummy;
  asm volatile("global_load_dword %0, %1, %2 sc1" : "=v"(epoch) : "v"(0), "s"(ws + WS_EPOCH) : "memory");
  for (int q = 0; q < MALL_DELAY; ++q) __builtin_amdgcn_s_sleep(4);
  asm volatile("global_load_dword %0, %1, off" : "=v"(wdummy) : "v"(warm) : "memory");
  const int wm = w >> 1, wn = w & 1;
  const int m = lane & 15, g = lane >> 4, q4 = (lane & 15) >> 2, p4 = lane & 3;
  const unsigned xb[2] = {(unsigned)((64 * wm + m) * 128 + 16 * ((g) ^ (m >> 1))), (unsigned)((64 * wm + m) * 128 + 16 * ((4 + g) ^ (m >> 1)))};
  unsigned wq[4], wf[4];
#pragma unroll
  for (int tl = 0; tl < 4; ++tl) {
    wq[tl] = (8 * g + q4) * 192 + 32 * ((3 * wn + (tl < 3 ? tl : 0)) ^ (g & 1)) + 8 * p4;
    wf[tl] = (8 * g + q4) * 256 + 32 * ((4 * wn + tl) ^ (q4 | ((g & 1) << 2))) + 8 * p4;
  }
  f32x4 aq[4][3], af[4][4];
#pragma unroll
  for (int a = 0; a < 4; ++a) {
#pragma unroll
    for (int t = 0; t < 3; ++t) aq[a][t] = (f32x4){0.f, 0.f, 0.f, 0.f};
#pragma unroll
    for (int t = 0; t < 4; ++t) af[a][t] = (f32x4){0.f, 0.f, 0.f, 0.f};
  }
  bf16x8 xf[2][4]; s16x4 wl[2][4], wh[2][4];
  const int tb0 = 4 * i + 2 * wm;
  raw_barrier();
  STAMPW(1);
#pragma unroll
  for (int r = 0; r < 4; ++r) xf[0][r] = *(LAS const bf16x8*)(lds + A_X + xb[0] + r * 2048);
#pragma unroll
  for (int tl = 0; tl < PassCfg<0>::NT; ++tl) { LAS const char* p = lds + A_W + (FFIRST ? wf[tl] : wq[tl]); wl[0][tl] = ldtr(p); wh[0][tl] = ldtr(p + 4 * PassCfg<0>::ROWB); }
  SB();
  auto hook = [&](auto C) {
    if constexpr (C == 0) { wait_vm<2 * PassCfg<0>::NT>(); asm volatile("" : "+v"(epoch), "+v"(wdummy)); if (w == 0) put_tag(lds, epoch, lane); }
    if constexpr (C == PUB_CHUNK) publish(lds, CTL_APUB0, 4u, ws, (FFIRST ? FLAG_H : 0) + i * 14 + c, lds_ld(lds, CTL_TAG), lane);
  };
  if constexpr (FFIRST) {
    a_pass<0>(lds, af, xf, wl, wh, xb, wf, wq, [](auto) {});
    a_store<true, 4>(af, ws, tb0, 8 * c + 4 * wn, lane);
  } else {
    a_pass<0>(lds, aq, xf, wl, wh, xb, wq, wf, [](auto) {});
    a_store<false, 3>(aq, ws, tb0, 6 * c + 3 * wn, lane);
  }
  SB();
  if constexpr (FFIRST) a_pass<1>(lds, aq, xf, wl, wh, xb, wq, wq, hook); else a_pass<1>(lds, af, xf, wl, wh, xb, wf, wf, hook);
  STAMPW(2);
  wait_lgkm0();
  if (lane == 0) __hip_atomic_fetch_add((LAS unsigned*)(lds + CTL_CDONE), 1u, __ATOMIC_RELAXED, __HIP_MEMORY_SCOPE_WORKGROUP);
  if constexpr (FFIRST) a_store<false, 3>(aq, ws, tb0, 6 * c + 3 * wn, lane); else a_store<true, 4>(af, ws, tb0, 8 * c + 4 * wn, lane);
  publish(lds, CTL_APUB1, 4u, ws, (FFIRST ? 0 : FLAG_H) + i * 14 + c, lds_ld(lds, CTL_TAG), lane);
}

DI void gate_rows(const char* x, const char* gw, const char* w2, const char* wo, char* ws, int gi, int part4, int w, int lane) {
  const int row = gi * 64 + w * 16 + (lane >> 2), part = lane & 3;
  float s = 0.f;
#pragma unroll
  for (int k = 0; k < 14; ++k) {
    const u32x4 xv = *(const u32x4*)(x + (size_t)row * 896 + k * 64 + part * 16);
    const u32x4 gv = *(const u32x4*)(gw + k * 64 + part * 16);
#pragma unroll
    for (int e = 0; e < 4; ++e) {
      s += __uint_as_float(xv[e] << 16) * __uint_as_float(gv[e] << 16);
      s += __uint_as_float(xv[e] & 0xffff0000u) * __uint_as_float(gv[e] & 0xffff0000u);
    }
  }
  s += __shfl_xor(s, 1); s += __shfl_xor(s, 2);
  if (part == 0) __hip_atomic_store((GAS float*)(ws + WS_G) + row, 1.f / (1.f + __expf(-s)), __ATOMIC_RELAXED, __HIP_MEMORY_SCOPE_AGENT);
}
DI void warm_proj(const char* x, const char* wqkv, const char* w1, int r, int c2, int part4, int t256) {
  unsigned acc = 0;
  const int t = part4 * 256 + t256;
#pragma unroll
  for (int k = 0; k < 2; ++k) { const int e = k * 1024 + t; acc ^= *(const unsigned*)(x + (size_t)(512 * r + (e >> 2)) * 896 + (3 + (e & 3)) * 128); }
#pragma unroll
  for (int k = 0; k < 3; ++k) { const int e = k * 1024 + t, row = 192 + e / 12, l = e % 12, o = (2 * (l >> 2) + c2) * 448; acc ^= *(const unsigned*)(wqkv + (size_t)row * 2688 + (o & ~127) + (l & 3) * 128); }
#pragma unroll
  for (int k = 0; k < 4; ++k) { const int e = k * 1024 + t; if (e < 3584) acc ^= *(const unsigned*)(w1 + (size_t)(192 + e / 14) * 3584 + c2 * 1792 + (e % 14) * 128); }
  asm volatile("" :: "v"(acc));
}
DI void warm_w2(LAS char* lds, const char* w2, const char* wo, int part4, int t256) {
  constexpr int PER_STEP = 4 * 16 * 7, NL = 36 * PER_STEP;
#pragma unroll 1
  for (int k = 0; k < (NL / 4 + 1023) / 1024; ++k) {
    if (lds_ld(lds, CTL_QRDY) != 0u) lds_wait_ge(lds, CTL_KV, 8u);
    unsigned v[4];
#pragma unroll
    for (int q = 0; q < 4; ++q) {
      const int e = min((k * 4 + q) * 1024 + part4 * 256 + t256, NL - 1);
      const int st = e / PER_STEP, r = e % PER_STEP, wv = r / 112, row = (r % 112) / 7, ln = r % 7;
      const char* src = st < 28 ? w2 + (size_t)(448 * wv + 16 * st + row) * 896 : wo + (size_t)min(112 * wv + 14 * (st - 28) + row, 447) * 896;
      v[q] = *(const unsigned*)(src + ln * 128);
    }
    asm volatile("" :: "v"(v[0]), "v"(v[1]), "v"(v[2]), "v"(v[3]));
  }
}

DI void b_attention(LAS char* lds, char* ws, int i, int h, int w4, int lane, int ig   STAMP_PARAM) {
  const unsigned lds0 = (unsigned)(size_t)lds;
  const unsigned tag = lds_ld(lds, CTL_TAG);
  const int u0 = (h & 1) ? (28 * h - 4) / 8 : (28 * h) / 8;
  const int hh = lane >> 5, ql = lane & 31;
  const int tbq = 4 * i + w4;
  __builtin_amdgcn_s_setprio(3);
  unsigned mf[16], mp[8];
#pragma unroll
  for (int r = 0; r < 16; ++r) mf[r] = ((r & 7) + 16 * (r >> 3) + 8 * hh > ql) ? 0xffffffffu : 0u;
#pragma unroll
  for (int k = 0; k < 8; ++k) mp[k] = (mf[2 * k] & 0xffffu) | (mf[2 * k + 1] & 0xffff0000u);
  const int sw = (ql & 0x13) | ((ql & 4) << 1) | ((ql & 8) >> 1);
  unsigned kp = lds0 + KV_K + w4 * 2048 + hh * 512 + sw * 16, vp0, vp1;
  {
    const int q4 = (lane & 15) >> 2, gam = 4 * ((lane >> 4) & 1) + (lane & 3);
    const int n = (4 * (h & 1) + 4 * gam) & 31, unit = n >> 3, half = (n >> 2) & 1;
    vp0 = lds0 + KV_V + w4 * 2048 + unit * 512 + (((8 * hh + q4) ^ (4 * unit)) * 16) + half * 8;
    vp1 = lds0 + KV_V + w4 * 2048 + unit * 512 + (((8 * hh + 4 + q4) ^ (4 * unit)) * 16) + half * 8;
  }
  unsigned kvo = lane * 16, vv0 = hh * 512 + ((ql ^ (4 * hh)) * 16), vv1 = hh * 512 + ((ql ^ (4 * (2 + hh))) * 16);
#pragma unroll
  for (int r = 0; r < 16; ++r) asm volatile("" : "+v"(mf[r]));
#pragma unroll
  for (int k = 0; k < 8; ++k) asm volatile("" : "+v"(mp[k]));
  asm volatile("" : "+v"(kp), "+v"(vp0), "+v"(vp1), "+v"(kvo), "+v"(vv0), "+v"(vv1));
  if (w4 == 0) {
    const int l = lane & 15, tlo = u0 >> 1, thi = (u0 + 3) >> 1;
    int f;
    if (l < 14) {
      const int sec = l < 2 ? 0 : (l < 8 ? 1 : 2), k = l < 2 ? l : (l < 8 ? l - 2 : l - 8);
      f = max(i - (k >> 1), 0) * 14 + (28 * sec + ((k & 1) ? thi : tlo)) / 6;
    } else f = FLAG_G + 2 * i + (l - 14);
    poll_flags(ws, f, tag);
    if (lane == 0) lds_st(lds, CTL_QRDY, 1u);
  } else {
    lds_wait_ge(lds, CTL_QRDY, 1u);
  }
  STAMPW(3);
  u32x4 q0, q1; float gq;
#pragma unroll
  for (int bq = 0; bq < 3; ++bq) {
    const int bb = 3 * w4 + bq, tb = max(4 * i - 8 + bb, 0);
    const char* kb = ws + WS_QKV + (size_t)tb * QKV_TB + 28 * 1024 + u0 * 512;
    glds16_dev(kb, kvo, lds0 + KV_K + bb * 2048);
    glds16_dev(kb + 1024, kvo, lds0 + KV_K + bb * 2048 + 1024);
  }
  gld16_dev(q0, ws + WS_QKV + (size_t)tbq * QKV_TB + u0 * 512, kvo);
  gld16_dev(q1, ws + WS_QKV + (size_t)tbq * QKV_TB + (u0 + 2) * 512, kvo);
#pragma unroll
  for (int bq = 0; bq < 3; ++bq) {
    const int bb = 3 * w4 + bq, tb = max(4 * i - 8 + bb, 0);
    const char* vb = ws + WS_QKV + (size_t)tb * QKV_TB + 56 * 1024 + u0 * 512;
    glds16_dev(vb, vv0, lds0 + KV_V + bb * 2048);
    glds16_dev(vb + 1024, vv1, lds0 + KV_V + bb * 2048 + 1024);
  }
  asm volatile("global_load_dword %0, %1, %2 sc1" : "=v"(gq) : "v"(ql * 4), "s"(ws + WS_G + tbq * 128) : "memory");
  wait_vm<7>();
  asm volatile("" : "+v"(q0), "+v"(q1));
  typedef unsigned u32x2 __attribute__((ext_vector_type(2)));
  if (lane < 32) {
#pragma unroll
    for (int bq = 0; bq < 3; ++bq)
      *(LAS u32x2*)(lds + KV_K + (3 * w4 + bq) * 2048 + ((h & 1) ? 0 : 3 * 512 + 8) + lane * 16) = (u32x2){(4 * i - 8 + 3 * w4 + bq < 0) ? 0xfe96u : 0u, 0u};
  }
  wait_lgkm0();
  if (lane == 0) __hip_atomic_fetch_add((LAS unsigned*)(lds + CTL_KV), 1u, __ATOMIC_RELAXED, __HIP_MEMORY_SCOPE_WORKGROUP);
  if (h & 1) { if (hh == 0) { q0[0] = 0x3f80u; q0[1] = 0; } } else { if (hh == 1) { q1[2] = 0x3f80u; q1[3] = 0; } }
  lds_wait_ge(lds, CTL_KV, 4u);
  STAMPW(4);
  const bf16x8 qa = __builtin_bit_cast(bf16x8, q0), qb = __builtin_bit_cast(bf16x8, q1);
  typedef LAS const char* lp;
  auto kfrag = [&](int j, int s) { return *(LAS const bf16x8*)((lp)(size_t)kp + j * 2048 + s * 1024); };
  auto vfrag = [&](int j, int s) { return cat(ldtr((lp)(size_t)vp0 + j * 2048 + s * 256), ldtr((lp)(size_t)vp1 + j * 2048 + s * 256)); };
  const f32x16 zero = {};
  const float NEG = -3.0e38f;
  auto merge = [&](f32x16& a, const f32x16& b8) {
#pragma unroll
    for (int r = 0; r < 16; ++r) a[r] = __uint_as_float((mf[r] & __float_as_uint(a[r])) | (~mf[r] & __float_as_uint(b8[r])));
  };
  auto max8 = [&](const f32x16& s, int b, float& m0, float& m1) {
    m0 = fmaxf(fmaxf(m0, s[b]), s[b + 1]); m1 = fmaxf(fmaxf(m1, s[b + 2]), s[b + 3]);
    m0 = fmaxf(fmaxf(m0, s[b + 4]), s[b + 5]); m1 = fmaxf(fmaxf(m1, s[b + 6]), s[b + 7]); };
  constexpr auto blk = [](int n) constexpr { return n == 0 ? 0 : (n == 1 ? 8 : n - 1); };
  f32x16 sv[3];
  bf16x8 kf[3][2];
  float m0 = NEG, m1 = NEG;
  kf[0][0] = kfrag(0, 0); kf[0][1] = kfrag(0, 1); kf[1][0] = kfrag(8, 0); kf[1][1] = kfrag(8, 1);
  SB();
  static_for<0, 10>([&](auto N) {
    constexpr int n = N;
    if constexpr (n < 9) sv[n % 3] = MFMA32(kf[n % 3][0], qa, zero);
    if constexpr (n + 2 < 11) { constexpr int nb = blk((n + 2) % 9); kf[(n + 2) % 3][0] = kfrag(nb, 0); kf[(n + 2) % 3][1] = kfrag(nb, 1); }
    SB();
    if constexpr (n == 2) merge(sv[0], sv[1]);
    if constexpr (n == 2) max8(sv[0], 0, m0, m1);
    if constexpr (n >= 3) max8(sv[(n - 1) % 3], 0, m0, m1);
    SB();
    if constexpr (n < 9) sv[n % 3] = MFMA32(kf[n % 3][1], qb, sv[n % 3]);
    SB();
    if constexpr (n == 2) max8(sv[0], 8, m0, m1);
    if constexpr (n >= 3) max8(sv[(n - 1) % 3], 8, m0, m1);
    SB();
  });
  float mx = fmaxf(m0, m1);
  { auto rr = __builtin_amdgcn_permlane32_swap(__float_as_uint(mx), __float_as_uint(mx), false, false);
    mx = fmaxf(__uint_as_float(rr[0]), __uint_as_float(rr[1])); }
  f32x16 negm;
#pragma unroll
  for (int r = 0; r < 16; ++r) negm[r] = -mx;
  wait_vm<0>();
  asm volatile("" : "+v"(gq));
  if (lane < 32) {
#pragma unroll
    for (int bq = 0; bq < 3; ++bq) *(LAS u32x2*)(lds + KV_V + (3 * w4 + bq) * 2048 + ((h & 1) ? 0 : 3 * 512 + 8) + lane * 16) = (u32x2){0x3f803f80u, 0x3f803f80u};
  }
  wait_lgkm0();
  if (lane == 0) __hip_atomic_fetch_add((LAS unsigned*)(lds + CTL_KV), 1u, __ATOMIC_RELAXED, __HIP_MEMORY_SCOPE_WORKGROUP);
  f32x16 o = zero;
  sv[0] = MFMA32(kf[0][0], qa, negm); sv[0] = MFMA32(kf[0][1], qb, sv[0]);
  kf[2][0] = kfrag(1, 0); kf[2][1] = kfrag(1, 1);
  sv[1] = MFMA32(kf[1][0], qa, negm); sv[1] = MFMA32(kf[1][1], qb, sv[1]);
  kf[0][0] = kfrag(2, 0); kf[0][1] = kfrag(2, 1);
  lds_wait_ge(lds, CTL_KV, 8u);
  STAMPW(5);
  bf16x8 vf[2][2], v8[2];
  vf[0][0] = vfrag(0, 0); vf[0][1] = vfrag(0, 1); v8[0] = vfrag(8, 0); v8[1] = vfrag(8, 1);
  SB();
  {
    sv[2] = MFMA32(kf[2][0], qa, negm);
    vf[1][0] = vfrag(1, 0); vf[1][1] = vfrag(1, 1);
    SB();
    merge(sv[0], sv[1]);
    SB();
    sv[2] = MFMA32(kf[2][1], qb, sv[2]);
    kf[1][0] = kfrag(3, 0); kf[1][1] = kfrag(3, 1);
    SB();
    u32x4 pa, pb;
#pragma unroll
    for (int k = 0; k < 4; ++k) pa[k] = pk2(__builtin_amdgcn_exp2f(sv[0][2 * k]), __builtin_amdgcn_exp2f(sv[0][2 * k + 1]));
    SB();
    o = MFMA32(vf[0][0], __builtin_bit_cast(bf16x8, (u32x4){pa[0] & mp[0], pa[1] & mp[1], pa[2] & mp[2], pa[3] & mp[3]}), o);
    SB();
#pragma unroll
    for (int k = 0; k < 2; ++k) pb[k] = pk2(__builtin_amdgcn_exp2f(sv[0][8 + 2 * k]), __builtin_amdgcn_exp2f(sv[0][9 + 2 * k]));
    SB();
    o = MFMA32(v8[0], __builtin_bit_cast(bf16x8, (u32x4){pa[0] & ~mp[0], pa[1] & ~mp[1], pa[2] & ~mp[2], pa[3] & ~mp[3]}), o);
    SB();
#pragma unroll
    for (int k = 2; k < 4; ++k) pb[k] = pk2(__builtin_amdgcn_exp2f(sv[0][8 + 2 * k]), __builtin_amdgcn_exp2f(sv[0][9 + 2 * k]));
    SB();
    o = MFMA32(vf[0][1], __builtin_bit_cast(bf16x8, (u32x4){pb[0] & mp[4], pb[1] & mp[5], pb[2] & mp[6], pb[3] & mp[7]}), o);
    SB();
    o = MFMA32(v8[1], __builtin_bit_cast(bf16x8, (u32x4){pb[0] & ~mp[4], pb[1] & ~mp[5], pb[2] & ~mp[6], pb[3] & ~mp[7]}), o);
    SB();
  }
  static_for<1, 8>([&](auto J) {
    constexpr int j = J;
    f32x16& S = sv[(j + 1) % 3];
    u32x4 pa, pb;
    if constexpr (j + 1 < 8) sv[(j + 2) % 3] = MFMA32(kf[(j + 2) % 3][0], qa, negm);
    if constexpr (j + 1 < 8) { vf[(j + 1) & 1][0] = vfrag(j + 1, 0); vf[(j + 1) & 1][1] = vfrag(j + 1, 1); }
    SB();
    pa[0] = pk2(__builtin_amdgcn_exp2f(S[0]), __builtin_amdgcn_exp2f(S[1])); pa[1] = pk2(__builtin_amdgcn_exp2f(S[2]), __builtin_amdgcn_exp2f(S[3]));
    SB();
    if constexpr (j + 1 < 8) sv[(j + 2) % 3] = MFMA32(kf[(j + 2) % 3][1], qb, sv[(j + 2) % 3]);
    if constexpr (j + 3 < 8) { kf[(j + 1) % 3][0] = kfrag(j + 3, 0); kf[(j + 1) % 3][1] = kfrag(j + 3, 1); }
    SB();
    pa[2] = pk2(__builtin_amdgcn_exp2f(S[4]), __builtin_amdgcn_exp2f(S[5])); pa[3] = pk2(__builtin_amdgcn_exp2f(S[6]), __builtin_amdgcn_exp2f(S[7]));
    pb[0] = pk2(__builtin_amdgcn_exp2f(S[8]), __builtin_amdgcn_exp2f(S[9]));
    SB();
    o = MFMA32(vf[j & 1][0], __builtin_bit_cast(bf16x8, pa), o);
    SB();
    pb[1] = pk2(__builtin_amdgcn_exp2f(S[10]), __builtin_amdgcn_exp2f(S[11])); pb[2] = pk2(__builtin_amdgcn_exp2f(S[12]), __builtin_amdgcn_exp2f(S[13]));
    pb[3] = pk2(__builtin_amdgcn_exp2f(S[14]), __builtin_amdgcn_exp2f(S[15]));
    SB();
    o = MFMA32(vf[j & 1][1], __builtin_bit_cast(bf16x8, pb), o);
    SB();
  });
  STAMPW(6);
  float l;
  { auto rr = __builtin_amdgcn_permlane32_swap(__float_as_uint(o[12]), __float_as_uint(o[12]), false, false); l = __uint_as_float(rr[1]); }
  const float scl = gq * __builtin_amdgcn_rcpf(l);
  u32x4 f0, f1;
  f0[0] = pk2(o[0] * scl, o[1] * scl); f0[1] = pk2(o[2] * scl, o[3] * scl); f0[2] = pk2(o[4] * scl, o[5] * scl); f0[3] = pk2(o[6] * scl, o[7] * scl);
  f1[0] = pk2(o[8] * scl, o[9] * scl); f1[1] = pk2(o[10] * scl, o[11] * scl); f1[2] = pk2(o[12] * scl, o[13] * scl); f1[3] = pk2(o[14] * scl, o[15] * scl);
  if (hh) { f1[2] = 0; f1[3] = 0; }
  {
    const auto rs = __builtin_amdgcn_make_buffer_rsrc((void*)ws, 0, (unsigned)WS_END, 0x00020000);
    const unsigned off = WS_O + (tbq * 16 + h) * 2048 + lane * 16;
    __builtin_amdgcn_raw_buffer_store_b128(f0, rs, off, 0, 16);
    __builtin_amdgcn_raw_buffer_store_b128(f1, rs, off + 1024, 0, 16);
  }
  publish(lds, CTL_OPUB, 4u, ws, FLAG_O + i * 16 + h, tag, lane);
  STAMPW(7);
  if (w4 == 0 && ig >= 0) {
    poll_flags(ws, FLAG_O + ig * 16 + (lane & 15), tag);
    if (lane == 0) lds_st(lds, CTL_ORDY, 1u);
    STAMPW(8);
  }
}

struct CW {
  const char* wsrc; const char* osrc; unsigned ring0, chs, wv; int orow, w;
  DI CW(LAS char* lds, const char* w2, const char* wo, int ctile, int w_, int lane) {
    w = w_; ring0 = (unsigned)(size_t)lds + RING0 + w * RING_STRIDE;
    wsrc = w2 + (size_t)(448 * w) * 896 + ctile * 128; osrc = wo + ctile * 128;
    chs = 16 * ((lane & 7) ^ (4 * ((lane >> 4) & 1))); wv = (lane >> 3) * 896 + chs; orow = ((lane >> 3) & 3) + 8 * (lane >> 5);
  }
  template <int s, int d> DI void issue() const {
    const unsigned lb = ring0 + (s % C_D) * 2048 + d * 1024;
    if constexpr (s < 28) glds16(wsrc + (size_t)(16 * s + 8 * d) * 896, wv, lb);
    else if constexpr (s < 36) {
      constexpr int o = s - 28;
      const int rb = 28 * (4 * w + (o >> 1)) + 16 * (o & 1);
      glds16(osrc, min(rb + orow + 4 * d, 447) * 896 + chs, lb);
    }
  }
};
constexpr int c_nw(int t) { return (t >= C_P && t + C_D < 36) ? 2 : 0; }
constexpr int c_nh(int t) { return (t + C_H < 36) ? 2 : 0; }
constexpr int c_allow(int s) {
  int n = 0;
  if (s < C_H) { n += 2 * (C_H - 1 - s); for (int t = 0; t < s; ++t) n += c_nw(t) + c_nh(t); }
  else { for (int t = s - C_H + 1; t < s; ++t) n += c_nw(t) + c_nh(t); }
  if (s + 1 < 36 && s + 1 - C_D >= C_P) { int k = c_nh(s + 1 - C_D); for (int t = s + 2 - C_D; t < s; ++t) k += c_nw(t) + c_nh(t); if (k < n) n = k; }
  return n;
}
struct CRead {
  LAS char* ring; unsigned tb_0, tb_1;
  DI CRead(LAS char* lds, int w, int lane) {
    ring = lds + RING0 + w * RING_STRIDE;
    const int hh = lane >> 5, blk = (lane >> 4) & 1, q4 = (lane & 15) >> 2, p4 = lane & 3;
    tb_0 = (8 * hh + q4) * 128 + 16 * ((0 + 2 * blk + (p4 >> 1)) ^ (4 * ((q4 >> 1) & 1))) + 8 * (p4 & 1);
    tb_1 = (8 * hh + q4) * 128 + 16 * ((4 + 2 * blk + (p4 >> 1)) ^ (4 * ((q4 >> 1) & 1))) + 8 * (p4 & 1);
  }
  template <int s, int ct> DI bf16x8 rd() const { LAS const char* b = ring + (s % C_D) * 2048 + (ct ? tb_1 : tb_0); return cat(ldtr(b), ldtr(b + 512)); }
};
DI void c_fill_take(LAS char* lds, const char* w2, const char* wo, int ctile, int w, int lane, bf16x8 (&wp)[C_P][2]) {
  const CW cw(lds, w2, wo, ctile, w, lane);
  const CRead cr(lds, w, lane);
  lds_wait_ge(lds, CTL_CDONE, 4u);
  static_for<0, C_D>([&](auto S) { cw.template issue<S, 0>(); cw.template issue<S, 1>(); });
  static_for<0, C_P>([&](auto S) {
    constexpr int s = S;
    wait_vm<2 * (C_D - 1)>();
    wp[s][0] = cr.template rd<s, 0>(); wp[s][1] = cr.template rd<s, 1>();
    wait_lgkm0();
    cw.template issue<s + C_D, 0>(); cw.template issue<s + C_D, 1>();
  });
}
DI void c_gemm(LAS char* lds, const char* w2, const char* wo, char* ws, int rt, int ctile, int w, int lane, const bf16x8 (&wp)[C_P][2], f32x16 (&acc)[2][2] STAMP_PARAM) {
  const CW cw(lds, w2, wo, ctile, w, lane);
  const CRead cr(lds, w, lane);
  const unsigned tag = lds_ld(lds, CTL_TAG);
  const int tb0 = 2 * rt;
  const char* hsrc = ws + WS_H + ((size_t)tb0 * 112 + 28 * w) * 1024;
  const char* ofr = ws + WS_O + ((size_t)tb0 * 16 + 4 * w) * 2048;
  const unsigned hv = lane * 16;
  u32x4 hr[C_H][2];
  auto issh = [&](auto S, auto Dn) {
    constexpr int s = S, d = Dn;
    if constexpr (s < 28) gld16_dev(hr[s % C_H][d], hsrc + s * 1024 + d * 112 * 1024, hv);
    else if constexpr (s < 36) gld16_dev(hr[s % C_H][d], ofr + (s - 28) * 1024 + d * 16 * 2048, hv);
  };
  bf16x8 wf[2][2];
  {
    const int l = lane & 7;
    poll_flags(ws, l < 4 ? FLAG_H + (rt >> 1) * 14 + (28 * w) / 8 + l : FLAG_G + rt, tag);
  }
  STAMPW(4);
  float g0, g1;
  asm volatile("global_load_dword %0, %2, %3 sc1\n\tglobal_load_dword %1, %2, %3 offset:128 sc1" : "=&v"(g0), "=&v"(g1) : "v"((lane & 31) * 4), "s"(ws + WS_G + tb0 * 128) : "memory");
  static_for<0, C_H>([&](auto S) { issh(S, IC<0>{}); issh(S, IC<1>{}); });
  static_for<0, 36>([&](auto S) {
    constexpr int s = S, buf = s & 1, slot = s % C_H;
    wait_lgkm0();
    constexpr int allow = c_allow(s);
    wait_vm<allow>();
    if constexpr (s == 0) STAMPW(5);
    asm volatile("" : "+v"(hr[slot][0]), "+v"(hr[slot][1]));
    const bf16x8 h0 = __builtin_bit_cast(bf16x8, hr[slot][0]), h1 = __builtin_bit_cast(bf16x8, hr[slot][1]);
    if constexpr (s == 28) {
      asm volatile("" : "+v"(g0), "+v"(g1));
      const float s0 = 1.f - g0, s1 = 1.f - g1;
#pragma unroll
      for (int ct = 0; ct < 2; ++ct) { acc[ct][0] *= s0; acc[ct][1] *= s1; }
    }
    if constexpr (s + C_H == 28) { lds_wait_ge(lds, CTL_ORDY, 1u);
 STAMPW(7);
 }
    SB();
    const bf16x8 w0 = s < C_P ? wp[s < C_P ? s : 0][0] : wf[buf][0], w1 = s < C_P ? wp[s < C_P ? s : 0][1] : wf[buf][1];
    constexpr bool rd = (s + 1 >= C_P && s + 1 < 36);
    acc[0][0] = MFMA32(w0, h0, acc[0][0]);
    if constexpr (rd) wf[buf ^ 1][0] = cr.template rd<s + 1, 0>();
    if constexpr (c_nw(s)) cw.template issue<s + C_D, 0>();
    SB();
    acc[0][1] = MFMA32(w0, h1, acc[0][1]);
    if constexpr (rd) wf[buf ^ 1][1] = cr.template rd<s + 1, 1>();
    if constexpr (c_nw(s)) cw.template issue<s + C_D, 1>();
    SB();
    acc[1][0] = MFMA32(w1, h0, acc[1][0]); SB();
    acc[1][1] = MFMA32(w1, h1, acc[1][1]); SB();
    if constexpr (c_nh(s)) { issh(IC<s + C_H>{}, IC<0>{}); issh(IC<s + C_H>{}, IC<1>{}); }
    SB();
  });
  STAMPW(8);
}

__global__ __launch_bounds__(512, 2) void fused_block(const bf16* __restrict__ x, const bf16* __restrict__ wqkv, const bf16* __restrict__ wo, const bf16* __restrict__ w1,
                                                      const bf16* __restrict__ w2, const bf16* __restrict__ gw, char* __restrict__ ws, bf16* __restrict__ out) {
  __shared__ __attribute__((aligned(16))) char smem[LDS_BYTES];
  LAS char* lds = (LAS char*)smem;
  unsigned long long pc0;
  asm volatile("s_getpc_b64 %0" : "=s"(pc0));
  const int b = blockIdx.x, xcd = b & 7, idx = b >> 3, r = xcd >> 1, c2 = xcd & 1;
  const int tid = threadIdx.x, lane = tid & 63;
  const int w = __builtin_amdgcn_readfirstlane(tid >> 6);
  STAMP_DECL;
  STAMPW(0);
  const bool tile = idx < 28;
  const int i = 4 * r + idx / 7, cc = idx % 7;
  const int c = 7 * c2 + cc;
  const int rt = 8 * r + 4 * c2 + idx / 7, ctile = idx % 7;
  if (__builtin_expect(w >= 4, 1)) {
    if (__builtin_expect(tile, 1)) {
      a_loader(lds, (const char*)x + (size_t)i * 128 * 896, (const char*)w1 + c * 256, (const char*)wqkv + c * 192, w - 4 STAMP_ARG);
    } else {
      {
        const char* base = (const char*)(pc0 & ~255ull);
        const int t = (idx - 28) * 256 + (tid - 256);
        unsigned v = 0;
        if (t * 128 < CODEWARM) v = *(const volatile unsigned*)(base + t * 128);
        asm volatile("" :: "v"(v));
      }
      raw_barrier();
    }
    STAMPW(2);
    b_attention(lds, ws, idx >> 1, 2 * xcd + (idx & 1), w - 4, lane, tile ? (rt >> 1) : -1 STAMP_ARG);
  } else if (tile) {
    const int tb = (8 * r + 4 * c2 + idx / 7) * 7 + cc;
    a_compute(lds, i, c, w, ws, warm_line(tb * 256 + tid, (const char*)x, (const char*)wqkv, (const char*)w1, (const char*)w2, (const char*)wo) STAMP_ARG);
    bf16x8 wp[C_P][2];
    STAMPW(3);
    c_fill_take(lds, (const char*)w2, (const char*)wo, ctile, w, lane, wp);
    f32x16 acc[2][2] = {};
    c_gemm(lds, (const char*)w2, (const char*)wo, ws, rt, ctile, w, lane, wp, acc STAMP_ARG);
    wait_lgkm0();
    LAS char* red = lds + RING0 + w * RING_STRIDE;
    const int hh = lane >> 5;
#pragma unroll
    for (int ct = 0; ct < 2; ++ct)
#pragma unroll
      for (int tt = 0; tt < 2; ++tt)
#pragma unroll
        for (int rg = 0; rg < 4; ++rg) {
          const f32x4 v = {acc[ct][tt][4 * rg], acc[ct][tt][4 * rg + 1], acc[ct][tt][4 * rg + 2], acc[ct][tt][4 * rg + 3]};
          *(LAS f32x4*)(red + (32 * tt + (lane & 31)) * RED_STRIDE + (32 * ct + 8 * rg + 4 * hh) * 4) = v;
        }
  } else {
    if (w == 0) set_tag(lds, ws, lane);
    raw_barrier();
    const int gi = (idx - 28) + 4 * xcd;
    gate_rows((const char*)x, (const char*)gw, (const char*)w2, (const char*)wo, ws, gi, idx - 28, w, lane);
    publish(lds, CTL_APUB0, 4u, ws, FLAG_G + gi, lds_ld(lds, CTL_TAG), lane);
    warm_w2(lds, (const char*)w2, (const char*)wo, idx - 28, tid);
    STAMPW(5);
    if (b == 255 && w == 0) {
      const unsigned tag = lds_ld(lds, CTL_TAG);
      GAS unsigned* fl = flag_ptr(ws, lane);
      for (unsigned sp = 0; sp < (1u << 18); ++sp) {
        const unsigned v0 = __hip_atomic_load(fl, __ATOMIC_RELAXED, __HIP_MEMORY_SCOPE_AGENT), v1 = __hip_atomic_load(fl + 64 * 32, __ATOMIC_RELAXED, __HIP_MEMORY_SCOPE_AGENT);
        const unsigned v2 = __hip_atomic_load(fl + 128 * 32, __ATOMIC_RELAXED, __HIP_MEMORY_SCOPE_AGENT);
        const unsigned v3 = __hip_atomic_load(fl + (lane < 32 ? 192 : FLAG_G - 32) * 32, __ATOMIC_RELAXED, __HIP_MEMORY_SCOPE_AGENT);
        if (__all(v0 == tag && v1 == tag && v2 == tag && v3 == tag)) break;
        __builtin_amdgcn_s_sleep(16);
      }
      if (lane == 0) __hip_atomic_store((GAS unsigned*)(ws + WS_EPOCH), lds_ld(lds, CTL_EPOCH) + 1u, __ATOMIC_RELAXED, __HIP_MEMORY_SCOPE_AGENT);
    }
  }
  STAMPW(9);
  if (!tile) { STAMP_FLUSH(); return; }
  __syncthreads();
  STAMPW(10);
  {
    const int tok = tid >> 3, cg = tid & 7;
    f32x4 a = {0, 0, 0, 0}, c = {0, 0, 0, 0};
#pragma unroll
    for (int ww = 0; ww < 4; ++ww) {
      a += *(LAS const f32x4*)(lds + RING0 + ww * RING_STRIDE + tok * RED_STRIDE + cg * 32);
      c += *(LAS const f32x4*)(lds + RING0 + ww * RING_STRIDE + tok * RED_STRIDE + cg * 32 + 16);
    }
    const u32x4 v = {pk2(a[0], a[1]), pk2(a[2], a[3]), pk2(c[0], c[1]), pk2(c[2], c[3])};
    *(u32x4*)((char*)out + (size_t)(rt * 64 + tok) * 896 + ctile * 128 + cg * 16) = v;
  }
  STAMP_FLUSH();
}

extern "C" void kernel_launch(void* const* d_in, const int* in_sizes, int n_in, void* d_out, int out_size, void* d_ws, size_t ws_size, hipStream_t stream) {
  fused_block<<<256, 512, 0, stream>>>((const bf16*)d_in[0], (const bf16*)d_in[1], (const bf16*)d_in[2], (const bf16*)d_in[4], (const bf16*)d_in[5], (const bf16*)d_in[6],
                                       (char*)d_ws, (bf16*)d_out);
}
```
